```python
import torch

def _softmax(x, axis=-1):
    return torch.softmax(x, dim=axis)
def _gelu(x):
    return torch.nn.functional.gelu(x, approximate="tanh")
def _silu(x):
    return torch.nn.functional.silu(x)
def _sigmoid(x):
    return torch.sigmoid(x)
def _layernorm(x, axis=-1, eps=1e-5):
    m = x.mean(dim=axis, keepdim=True)
    v = x.var(dim=axis, keepdim=True, unbiased=False)
    return (x - m) / torch.sqrt(v + eps)
def _scatter_add(tgt, idx, src, axis=0):
    return tgt.clone().index_add_(axis, idx, src)
def _segment_sum(src, seg, n):
    out = torch.zeros((n,) + src.shape[1:], dtype=src.dtype)
    return out.index_add_(0, seg, src)
def _pad(x, pw):
    flat = tuple(v for pair in reversed(pw) for v in pair)
    return torch.nn.functional.pad(x, flat)

kernel_name = 'standalone__gqa_block_cached__decode'

def setup_inputs():

    _w = (lambda a: (lambda t: t.to(torch.bfloat16) if t.is_floating_point() else t)(torch.as_tensor(a)))
    B, Sq, D = 1, 1, 640
    Hq, Hkv, Sk, Dh = 32, 32, 32768, 128
    assert Hq % Hkv == 0, (Hq, Hkv)
    return {
        "x": _w((lambda *s: torch.randn(*s, dtype=torch.bfloat16) * 0.8)(B, Sq, D)),
        "k_cache": _w((lambda *s: torch.randn(*s, dtype=torch.bfloat16) * 0.8)(B, Hkv, Sk, Dh)),
        "v_cache": _w((lambda *s: torch.randn(*s, dtype=torch.bfloat16) * 0.05)(B, Hkv, Sk, Dh)),
        "wq": _w((lambda *s: torch.randn(*s, dtype=torch.bfloat16) * 0.8)(D, Hq * Dh) * (D ** -0.5)),
        "wo": _w((lambda *s: torch.randn(*s, dtype=torch.bfloat16) * 0.05)(Hq * Dh, D) * ((Hq * Dh) ** -0.5)),
    }

def reference(x, k_cache, v_cache, wq, wo):
    B, Sq, D = x.shape
    Hq, Dh = 32, 128
    Hkv = k_cache.shape[1]
    rep = Hq // Hkv
    q = (lambda x, *d: x.permute(*d))((x @ wq).reshape(B, Sq, Hq, Dh), 0, 2, 1, 3)
    k = (lambda x, n, axis: torch.repeat_interleave(x, n, dim=axis))(k_cache, rep, axis=1)
    v = (lambda x, n, axis: torch.repeat_interleave(x, n, dim=axis))(v_cache, rep, axis=1)
    scores = torch.einsum("bhqd,bhkd->bhqk", q, k) * (Dh ** -0.5)
    p = _softmax(scores, axis=-1)
    o = torch.einsum("bhqk,bhkd->bhqd", p, v)
    return (lambda x, *d: x.permute(*d))(o, 0, 2, 1, 3).reshape(B, Sq, Hq * Dh) @ wo
```

```cpp
#include <hip/hip_runtime.h>
#include <hip/hip_bf16.h>
#include <stdint.h>

typedef __attribute__((ext_vector_type(4))) unsigned u32x4;
typedef __attribute__((ext_vector_type(2))) __bf16 bf16x2;
typedef __attribute__((ext_vector_type(4))) float f32x4;

constexpr int D_MODEL = 640, H = 32, SK = 32768, DH = 128;
constexpr int NSPLIT = 8;
constexpr int KEYS_PER_SPLIT = SK / NSPLIT;
constexpr int NW = 8;

__device__ __forceinline__ float dot2(unsigned k, unsigned q, float acc) {
  return __builtin_amdgcn_fdot2_f32_bf16(__builtin_bit_cast(bf16x2, k), __builtin_bit_cast(bf16x2, q), acc, false);
}
template <int CTRL> __device__ __forceinline__ float dpp(float x) {
  return __builtin_bit_cast(float, __builtin_amdgcn_mov_dpp(__builtin_bit_cast(int, x), CTRL, 0xf, 0xf, true));
}
constexpr int XOR1 = 0xB1, XOR2 = 0x4E, XOR7 = 0x141, XOR8 = 0x128;
__device__ __forceinline__ float rowsum16(float x) {
  x += dpp<XOR8>(x); x += dpp<XOR7>(x); x += dpp<XOR2>(x); x += dpp<XOR1>(x); return x;
}
__device__ __forceinline__ float bf_lo(unsigned u) { return __builtin_bit_cast(float, u << 16); }
__device__ __forceinline__ float bf_hi(unsigned u) { return __builtin_bit_cast(float, u & 0xffff0000u); }
__device__ __forceinline__ unsigned short f2bf(float f) {
  __bf16 b = (__bf16)f; return __builtin_bit_cast(unsigned short, b);
}

__global__ void __launch_bounds__(256) qproj_kernel(const unsigned short* __restrict__ x, const unsigned short* __restrict__ wq, unsigned short* __restrict__ q) {
  __shared__ float red[16][128];
  const int h = blockIdx.x, t = threadIdx.x, c = t & 15, r = t >> 4;
  float acc[8];
#pragma unroll
  for (int j = 0; j < 8; ++j) acc[j] = 0.f;
  for (int d = r; d < D_MODEL; d += 16) {
    const float xv = bf_lo(x[d]);
    const u32x4 w = *reinterpret_cast<const u32x4*>(wq + (size_t)d * (H * DH) + h * DH + 8 * c);
#pragma unroll
    for (int j = 0; j < 4; ++j) { acc[2 * j] = fmaf(xv, bf_lo(w[j]), acc[2 * j]); acc[2 * j + 1] = fmaf(xv, bf_hi(w[j]), acc[2 * j + 1]); }
  }
#pragma unroll
  for (int j = 0; j < 8; ++j) red[r][8 * c + j] = acc[j];
  __syncthreads();
  if (t < 128) { float s = 0.f; for (int i = 0; i < 16; ++i) s += red[i][t]; q[h * DH + t] = f2bf(s); }
}

__global__ void __launch_bounds__(NW * 64) attn_kernel(const unsigned short* __restrict__ q, const unsigned short* __restrict__ kc, const unsigned short* __restrict__ vc,
                                                      float* __restrict__ pml, float* __restrict__ po) {
  __shared__ __attribute__((aligned(16))) float mrg[NW * 4][132];
  const int b = blockIdx.x, h = b / NSPLIT, s = b % NSPLIT;
  const int tid = threadIdx.x, lane = tid & 63, wave = __builtin_amdgcn_readfirstlane(tid >> 6), r = lane >> 4, c = lane & 15;
  constexpr float C2 = 0.08838834764831845f * 1.4426950408889634f;
  const u32x4 qv = *reinterpret_cast<const u32x4*>(q + h * DH + 8 * c);
  const size_t base = ((size_t)h * SK + (size_t)s * KEYS_PER_SPLIT) * DH + (size_t)lane * 8;
  const u32x4* kp = reinterpret_cast<const u32x4*>(kc + base);
  const u32x4* vp = reinterpret_cast<const u32x4*>(vc + base);
  constexpr int NT = KEYS_PER_SPLIT / 16 / NW;
  float m = -INFINITY, l = 0.f, acc[8];
#pragma unroll
  for (int j = 0; j < 8; ++j) acc[j] = 0.f;
  u32x4 Kn[4], Vn[4];
  {
    const int t0 = wave * 256;
#pragma unroll
    for (int i = 0; i < 4; ++i) { Kn[i] = __builtin_nontemporal_load(kp + t0 + i * 64); }
#pragma unroll
    for (int i = 0; i < 4; ++i) { Vn[i] = __builtin_nontemporal_load(vp + t0 + i * 64); }
  }
  for (int t = 0; t < NT; ++t) {
    u32x4 Kc[4], Vc[4];
#pragma unroll
    for (int i = 0; i < 4; ++i) { Kc[i] = Kn[i]; Vc[i] = Vn[i]; }
    if (t + 1 < NT) {
      const int t0 = ((t + 1) * NW + wave) * 256;
#pragma unroll
      for (int i = 0; i < 4; ++i) { Kn[i] = __builtin_nontemporal_load(kp + t0 + i * 64); }
#pragma unroll
      for (int i = 0; i < 4; ++i) { Vn[i] = __builtin_nontemporal_load(vp + t0 + i * 64); }
    }
    float sc[4];
#pragma unroll
    for (int i = 0; i < 4; ++i) {
      float d = 0.f;
#pragma unroll
      for (int k = 0; k < 4; ++k) d = dot2(Kc[i][k], qv[k], d);
      sc[i] = rowsum16(d) * C2;
    }
    const float mn = fmaxf(fmaxf(fmaxf(sc[0], sc[1]), fmaxf(sc[2], sc[3])), m);
    const float alpha = __builtin_amdgcn_exp2f(m - mn);
    m = mn;
    l *= alpha;
#pragma unroll
    for (int j = 0; j < 8; ++j) acc[j] *= alpha;
#pragma unroll
    for (int i = 0; i < 4; ++i) {
      const float p = __builtin_amdgcn_exp2f(sc[i] - m);
      l += p;
#pragma unroll
      for (int k = 0; k < 4; ++k) { acc[2 * k] = fmaf(p, bf_lo(Vc[i][k]), acc[2 * k]); acc[2 * k + 1] = fmaf(p, bf_hi(Vc[i][k]), acc[2 * k + 1]); }
    }
  }
  float* rec = mrg[wave * 4 + r];
  *reinterpret_cast<f32x4*>(rec + 4 + 8 * c) = f32x4{acc[0], acc[1], acc[2], acc[3]};
  *reinterpret_cast<f32x4*>(rec + 8 + 8 * c) = f32x4{acc[4], acc[5], acc[6], acc[7]};
  if (c == 0) { rec[0] = m; rec[1] = l; }
  __syncthreads();
  if (tid < 128) {
    float M = -INFINITY;
    for (int i = 0; i < NW * 4; ++i) M = fmaxf(M, mrg[i][0]);
    float L = 0.f, o = 0.f;
    for (int i = 0; i < NW * 4; ++i) { const float w = __builtin_amdgcn_exp2f(mrg[i][0] - M); L = fmaf(w, mrg[i][1], L); o = fmaf(w, mrg[i][4 + tid], o); }
    po[(size_t)b * DH + tid] = o;
    if (tid == 0) { pml[2 * b] = M; pml[2 * b + 1] = L; }
  }
}

__global__ void __launch_bounds__(1024) out_kernel(const float* __restrict__ pml, const float* __restrict__ po, const unsigned short* __restrict__ wo, unsigned short* __restrict__ out) {
  __shared__ float o_s[H * DH];
  __shared__ float red[16][64];
  const int tid = threadIdx.x;
  for (int i = tid; i < H * DH; i += 1024) {
    const int h = i >> 7, d = i & 127;
    float M = -INFINITY;
    for (int s = 0; s < NSPLIT; ++s) M = fmaxf(M, pml[2 * (h * NSPLIT + s)]);
    float L = 0.f, o = 0.f;
    for (int s = 0; s < NSPLIT; ++s) { const int b = h * NSPLIT + s; const float w = __builtin_amdgcn_exp2f(pml[2 * b] - M); L = fmaf(w, pml[2 * b + 1], L); o = fmaf(w, po[(size_t)b * DH + d], o); }
    o_s[i] = bf_lo(f2bf(o / L));
  }
  __syncthreads();
  const int col = blockIdx.x * 64 + (tid & 63), rg = tid >> 6;
  float a = 0.f;
  for (int k = rg; k < H * DH; k += 16) a = fmaf(o_s[k], bf_lo(wo[(size_t)k * D_MODEL + col]), a);
  red[rg][tid & 63] = a;
  __syncthreads();
  if (tid < 64) { float s = 0.f; for (int i = 0; i < 16; ++i) s += red[i][tid]; out[col] = f2bf(s); }
}

extern "C" void kernel_launch(void* const* d_in, const int* in_sizes, int n_in, void* d_out, int out_size, void* d_ws, size_t ws_size, hipStream_t stream) {
  const unsigned short* x = (const unsigned short*)d_in[0];
  const unsigned short* kc = (const unsigned short*)d_in[1];
  const unsigned short* vc = (const unsigned short*)d_in[2];
  const unsigned short* wq = (const unsigned short*)d_in[3];
  const unsigned short* wo = (const unsigned short*)d_in[4];
  unsigned short* q = (unsigned short*)d_ws;
  float* pml = (float*)((char*)d_ws + 16384);
  float* po = (float*)((char*)d_ws + 32768);
  qproj_kernel<<<H, 256, 0, stream>>>(x, wq, q);
  attn_kernel<<<H * NSPLIT, NW * 64, 0, stream>>>(q, kc, vc, pml, po);
  out_kernel<<<D_MODEL / 64, 1024, 0, stream>>>(pml, po, wo, (unsigned short*)d_out);
}
```

```cpp
#define NT_K 0
#define NT_V 1
#include <hip/hip_runtime.h>
#include <hip/hip_bf16.h>
#include <stdint.h>

typedef __attribute__((ext_vector_type(4))) unsigned u32x4;
typedef __attribute__((ext_vector_type(2))) __bf16 bf16x2;
typedef __attribute__((ext_vector_type(4))) float f32x4;

constexpr int D_MODEL = 640, H = 32, SK = 32768, DH = 128;
constexpr int NSPLIT = 8;
constexpr int KEYS_PER_SPLIT = SK / NSPLIT;
constexpr int NW = 8;

__device__ __forceinline__ float dot2(unsigned k, unsigned q, float acc) {
  return __builtin_amdgcn_fdot2_f32_bf16(__builtin_bit_cast(bf16x2, k), __builtin_bit_cast(bf16x2, q), acc, false);
}
template <int CTRL> __device__ __forceinline__ float dpp(float x) {
  return __builtin_bit_cast(float, __builtin_amdgcn_mov_dpp(__builtin_bit_cast(int, x), CTRL, 0xf, 0xf, true));
}
constexpr int XOR1 = 0xB1, XOR2 = 0x4E, XOR7 = 0x141, XOR8 = 0x128;
__device__ __forceinline__ float rowsum16(float x) {
  x += dpp<XOR8>(x); x += dpp<XOR7>(x); x += dpp<XOR2>(x); x += dpp<XOR1>(x); return x;
}
__device__ __forceinline__ float bf_lo(unsigned u) { return __builtin_bit_cast(float, u << 16); }
__device__ __forceinline__ float bf_hi(unsigned u) { return __builtin_bit_cast(float, u & 0xffff0000u); }
__device__ __forceinline__ unsigned short f2bf(float f) {
  __bf16 b = (__bf16)f; return __builtin_bit_cast(unsigned short, b);
}

__global__ void __launch_bounds__(256) qproj_kernel(const unsigned short* __restrict__ x, const unsigned short* __restrict__ wq, unsigned short* __restrict__ q) {
  __shared__ float red[16][128];
  const int h = blockIdx.x, t = threadIdx.x, c = t & 15, r = t >> 4;
  float acc[8];
#pragma unroll
  for (int j = 0; j < 8; ++j) acc[j] = 0.f;
  for (int d = r; d < D_MODEL; d += 16) {
    const float xv = bf_lo(x[d]);
    const u32x4 w = *reinterpret_cast<const u32x4*>(wq + (size_t)d * (H * DH) + h * DH + 8 * c);
#pragma unroll
    for (int j = 0; j < 4; ++j) { acc[2 * j] = fmaf(xv, bf_lo(w[j]), acc[2 * j]); acc[2 * j + 1] = fmaf(xv, bf_hi(w[j]), acc[2 * j + 1]); }
  }
#pragma unroll
  for (int j = 0; j < 8; ++j) red[r][8 * c + j] = acc[j];
  __syncthreads();
  if (t < 128) { float s = 0.f; for (int i = 0; i < 16; ++i) s += red[i][t]; q[h * DH + t] = f2bf(s); }
}

__global__ void __launch_bounds__(NW * 64) attn_kernel(const unsigned short* __restrict__ q, const unsigned short* __restrict__ kc, const unsigned short* __restrict__ vc,
                                                      float* __restrict__ pml, float* __restrict__ po) {
  __shared__ __attribute__((aligned(16))) float mrg[NW * 4][132];
  const int b = blockIdx.x, h = b / NSPLIT, s = b % NSPLIT;
  const int tid = threadIdx.x, lane = tid & 63, wave = __builtin_amdgcn_readfirstlane(tid >> 6), r = lane >> 4, c = lane & 15;
  constexpr float C2 = 0.08838834764831845f * 1.4426950408889634f;
  const u32x4 qv = *reinterpret_cast<const u32x4*>(q + h * DH + 8 * c);
  const size_t base = ((size_t)h * SK + (size_t)s * KEYS_PER_SPLIT) * DH + (size_t)lane * 8;
  const u32x4* kp = reinterpret_cast<const u32x4*>(kc + base);
  const u32x4* vp = reinterpret_cast<const u32x4*>(vc + base);
  constexpr int NT = KEYS_PER_SPLIT / 16 / NW;
  float m = -INFINITY, l = 0.f, acc[8];
#pragma unroll
  for (int j = 0; j < 8; ++j) acc[j] = 0.f;
  u32x4 Kn[4], Vn[4];
  {
    const int t0 = wave * 256;
#pragma unroll
    for (int i = 0; i < 4; ++i) { Kn[i] = NT_K ? __builtin_nontemporal_load(kp + t0 + i * 64) : kp[t0 + i * 64]; }
#pragma unroll
    for (int i = 0; i < 4; ++i) { Vn[i] = NT_V ? __builtin_nontemporal_load(vp + t0 + i * 64) : vp[t0 + i * 64]; }
  }
  for (int t = 0; t < NT; ++t) {
    u32x4 Kc[4], Vc[4];
#pragma unroll
    for (int i = 0; i < 4; ++i) { Kc[i] = Kn[i]; Vc[i] = Vn[i]; }
    if (t + 1 < NT) {
      const int t0 = ((t + 1) * NW + wave) * 256;
#pragma unroll
      for (int i = 0; i < 4; ++i) { Kn[i] = NT_K ? __builtin_nontemporal_load(kp + t0 + i * 64) : kp[t0 + i * 64]; }
#pragma unroll
      for (int i = 0; i < 4; ++i) { Vn[i] = NT_V ? __builtin_nontemporal_load(vp + t0 + i * 64) : vp[t0 + i * 64]; }
    }
    float sc[4];
#pragma unroll
    for (int i = 0; i < 4; ++i) {
      float d = 0.f;
#pragma unroll
      for (int k = 0; k < 4; ++k) d = dot2(Kc[i][k], qv[k], d);
      sc[i] = rowsum16(d) * C2;
    }
    const float mn = fmaxf(fmaxf(fmaxf(sc[0], sc[1]), fmaxf(sc[2], sc[3])), m);
    const float alpha = __builtin_amdgcn_exp2f(m - mn);
    m = mn;
    l *= alpha;
#pragma unroll
    for (int j = 0; j < 8; ++j) acc[j] *= alpha;
#pragma unroll
    for (int i = 0; i < 4; ++i) {
      const float p = __builtin_amdgcn_exp2f(sc[i] - m);
      l += p;
#pragma unroll
      for (int k = 0; k < 4; ++k) { acc[2 * k] = fmaf(p, bf_lo(Vc[i][k]), acc[2 * k]); acc[2 * k + 1] = fmaf(p, bf_hi(Vc[i][k]), acc[2 * k + 1]); }
    }
  }
  float* rec = mrg[wave * 4 + r];
  *reinterpret_cast<f32x4*>(rec + 4 + 8 * c) = f32x4{acc[0], acc[1], acc[2], acc[3]};
  *reinterpret_cast<f32x4*>(rec + 8 + 8 * c) = f32x4{acc[4], acc[5], acc[6], acc[7]};
  if (c == 0) { rec[0] = m; rec[1] = l; }
  __syncthreads();
  if (tid < 128) {
    float M = -INFINITY;
    for (int i = 0; i < NW * 4; ++i) M = fmaxf(M, mrg[i][0]);
    float L = 0.f, o = 0.f;
    for (int i = 0; i < NW * 4; ++i) { const float w = __builtin_amdgcn_exp2f(mrg[i][0] - M); L = fmaf(w, mrg[i][1], L); o = fmaf(w, mrg[i][4 + tid], o); }
    po[(size_t)b * DH + tid] = o;
    if (tid == 0) { pml[2 * b] = M; pml[2 * b + 1] = L; }
  }
}

__global__ void __launch_bounds__(1024) out_kernel(const float* __restrict__ pml, const float* __restrict__ po, const unsigned short* __restrict__ wo, unsigned short* __restrict__ out) {
  __shared__ float o_s[H * DH];
  __shared__ float red[16][64];
  const int tid = threadIdx.x;
  for (int i = tid; i < H * DH; i += 1024) {
    const int h = i >> 7, d = i & 127;
    float M = -INFINITY;
    for (int s = 0; s < NSPLIT; ++s) M = fmaxf(M, pml[2 * (h * NSPLIT + s)]);
    float L = 0.f, o = 0.f;
    for (int s = 0; s < NSPLIT; ++s) { const int b = h * NSPLIT + s; const float w = __builtin_amdgcn_exp2f(pml[2 * b] - M); L = fmaf(w, pml[2 * b + 1], L); o = fmaf(w, po[(size_t)b * DH + d], o); }
    o_s[i] = bf_lo(f2bf(o / L));
  }
  __syncthreads();
  const int col = blockIdx.x * 64 + (tid & 63), rg = tid >> 6;
  float a = 0.f;
  for (int k = rg; k < H * DH; k += 16) a = fmaf(o_s[k], bf_lo(wo[(size_t)k * D_MODEL + col]), a);
  red[rg][tid & 63] = a;
  __syncthreads();
  if (tid < 64) { float s = 0.f; for (int i = 0; i < 16; ++i) s += red[i][tid]; out[col] = f2bf(s); }
}

extern "C" void kernel_launch(void* const* d_in, const int* in_sizes, int n_in, void* d_out, int out_size, void* d_ws, size_t ws_size, hipStream_t stream) {
  const unsigned short* x = (const unsigned short*)d_in[0];
  const unsigned short* kc = (const unsigned short*)d_in[1];
  const unsigned short* vc = (const unsigned short*)d_in[2];
  const unsigned short* wq = (const unsigned short*)d_in[3];
  const unsigned short* wo = (const unsigned short*)d_in[4];
  unsigned short* q = (unsigned short*)d_ws;
  float* pml = (float*)((char*)d_ws + 16384);
  float* po = (float*)((char*)d_ws + 32768);
  qproj_kernel<<<H, 256, 0, stream>>>(x, wq, q);
  attn_kernel<<<H * NSPLIT, NW * 64, 0, stream>>>(q, kc, vc, pml, po);
  out_kernel<<<D_MODEL / 64, 1024, 0, stream>>>(pml, po, wo, (unsigned short*)d_out);
}
```

```cpp
#include <hip/hip_runtime.h>
#include <hip/hip_bf16.h>
#include <stdint.h>

typedef __attribute__((ext_vector_type(4))) unsigned u32x4;
typedef __attribute__((ext_vector_type(4))) float f32x4;
typedef __attribute__((ext_vector_type(2))) __bf16 bf16x2;
typedef __amdgpu_buffer_rsrc_t rsrc_t;
typedef __attribute__((address_space(1))) unsigned long long gu64;
typedef __attribute__((address_space(1))) unsigned gu32;
typedef __attribute__((address_space(3))) volatile unsigned lds_vu32;

#define X_YPRIO 1
#define TPC 2
#define LA 5
#define DBG_TS 0

constexpr int D_MODEL = 640, NH = 32, SK = 32768, DH = 128;
constexpr int NWS = 8;
#define NWD 4
constexpr int NTHR = (NWS + 1) * 64;
constexpr unsigned TB = 4096;
constexpr int TPB = 256;
#define N1 192
#define LOOK 8
#define NPRE 3
constexpr int CPB = (TPB - N1) / TPC;
constexpr int PC = 8 * CPB;
constexpr int MAXU = PC * TPC + 32;
constexpr unsigned HEADB = 8u << 20, KVB = 256u << 20;
constexpr unsigned NOTREADY = 0xffffffffu, ENDV = 0x80000000u;
constexpr unsigned SPIN_MAX = 1u << 16;

constexpr size_t WS_GEN = 0;
constexpr size_t WS_CNT = 4096;
constexpr size_t WS_QG = 16384;
constexpr size_t WS_PG = WS_QG + 32 * 8 * 1024;
constexpr size_t WS_YG = WS_PG + 32 * 8 * 1152;
constexpr size_t WS_DBG = 1u << 20;

constexpr int L_CNT1 = 0, L_CNT2 = 4, L_QFLAG = 8, L_QCNT = 12;
constexpr int L_TAB = 16;
constexpr int L_Q = NTHR * 4;
constexpr int L_ML = L_Q + 256;
constexpr int L_ML2 = L_ML + 64;
constexpr int L_REC = L_ML2 + 64;
constexpr int L_PG = L_REC + 8 * 128 * 4;
constexpr int L_YP = L_PG + 8 * 128 * 4;
constexpr int L_QP = L_YP + 8 * 128 * 4;
constexpr int L_WO = L_QP + 4 * 128 * 4;
constexpr int L_END = L_WO + 32768;
constexpr int LDS_BYTES = 84 * 1024;
static_assert(L_END <= LDS_BYTES, "lds");
static_assert(MAXU + 4 <= NTHR, "table init");

#define RLX_AGENT __ATOMIC_RELAXED, __HIP_MEMORY_SCOPE_AGENT

__device__ __forceinline__ float dot2(unsigned a, unsigned b, float acc) {
  return __builtin_amdgcn_fdot2_f32_bf16(__builtin_bit_cast(bf16x2, a), __builtin_bit_cast(bf16x2, b), acc, false);
}
template <int CTRL> __device__ __forceinline__ float dpp(float x) {
  return __builtin_bit_cast(float, __builtin_amdgcn_mov_dpp(__builtin_bit_cast(int, x), CTRL, 0xf, 0xf, true));
}
__device__ __forceinline__ float rowsum16(float x) {
  x += dpp<0x128>(x); x += dpp<0x141>(x); x += dpp<0x4E>(x); x += dpp<0xB1>(x); return x;
}
__device__ __forceinline__ float xrow_sum(float x) {
  auto s = __builtin_amdgcn_permlane16_swap(__float_as_uint(x), __float_as_uint(x), false, false);
  x = __uint_as_float(s[0]) + __uint_as_float(s[1]);
  auto t = __builtin_amdgcn_permlane32_swap(__float_as_uint(x), __float_as_uint(x), false, false);
  return __uint_as_float(t[0]) + __uint_as_float(t[1]);
}
__device__ __forceinline__ float xrow_max(float x) {
  auto s = __builtin_amdgcn_permlane16_swap(__float_as_uint(x), __float_as_uint(x), false, false);
  x = fmaxf(__uint_as_float(s[0]), __uint_as_float(s[1]));
  auto t = __builtin_amdgcn_permlane32_swap(__float_as_uint(x), __float_as_uint(x), false, false);
  return fmaxf(__uint_as_float(t[0]), __uint_as_float(t[1]));
}
__device__ __forceinline__ float bf_lo(unsigned u) { return __builtin_bit_cast(float, u << 16); }
__device__ __forceinline__ float bf_hi(unsigned u) { return __builtin_bit_cast(float, u & 0xffff0000u); }
__device__ __forceinline__ unsigned f2bf(float f) { __bf16 b = (__bf16)f; return (unsigned)__builtin_bit_cast(unsigned short, b); }
__device__ __forceinline__ u32x4 bld_nt(rsrc_t r, unsigned voff, unsigned soff) { return __builtin_amdgcn_raw_buffer_load_b128(r, voff, soff, 2); }
__device__ __forceinline__ void st_gran(unsigned char* p, unsigned tag, float v) {
  __hip_atomic_store((gu64*)p, ((unsigned long long)tag << 32) | __float_as_uint(v), RLX_AGENT);
}
__device__ __forceinline__ unsigned long long ld_gran(const unsigned char* p) { return __hip_atomic_load((gu64*)p, RLX_AGENT); }
__device__ __forceinline__ bool tag_ok(unsigned long long g, unsigned tag) { return (unsigned)(g >> 32) == tag; }
__device__ __forceinline__ float gval(unsigned long long g) { return __uint_as_float((unsigned)g); }

__device__ __forceinline__ unsigned lds_ticket(unsigned addr) {
  unsigned r, t0, t1;
  asm volatile("v_mov_b32 %1, %3\n\tv_mov_b32 %2, 1\n\ts_mov_b64 exec, 1\n\tds_add_rtn_u32 %1, %1, %2\n\ts_waitcnt lgkmcnt(0)\n\ts_mov_b64 exec, -1\n\tv_readfirstlane_b32 %0, %1"
               : "=s"(r), "=&v"(t0), "=&v"(t1) : "s"(addr) : "memory");
  return r;
}
__device__ __forceinline__ unsigned lds_rd(unsigned addr) {
  unsigned r, t0;
  asm volatile("v_mov_b32 %1, %2\n\tds_read_b32 %1, %1\n\ts_waitcnt lgkmcnt(0)\n\tv_readfirstlane_b32 %0, %1" : "=s"(r), "=&v"(t0) : "s"(addr) : "memory");
  return r;
}
#define WG_BARRIER() do { __builtin_amdgcn_sched_barrier(0); asm volatile("s_waitcnt lgkmcnt(0)\n\ts_barrier" ::: "memory"); __builtin_amdgcn_sched_barrier(0); } while (0)

#define TS(slot) do {} while (0)
#define TSDUMP() do {} while (0)

__device__ __forceinline__ void merge8(const unsigned char* ml, const float* o, int d, float& M, float& L, float& O) {
  const f32x4 m0 = *reinterpret_cast<const f32x4*>(ml), m1 = *reinterpret_cast<const f32x4*>(ml + 16);
  const f32x4 l0 = *reinterpret_cast<const f32x4*>(ml + 32), l1 = *reinterpret_cast<const f32x4*>(ml + 48);
  M = fmaxf(fmaxf(fmaxf(m0[0], m0[1]), fmaxf(m0[2], m0[3])), fmaxf(fmaxf(m1[0], m1[1]), fmaxf(m1[2], m1[3])));
  L = 0.f; O = 0.f;
#pragma unroll
  for (int w = 0; w < 8; ++w) {
    const float e = __builtin_amdgcn_exp2f((w < 4 ? m0[w & 3] : m1[w & 3]) - M);
    L = fmaf(e, w < 4 ? l0[w & 3] : l1[w & 3], L);
    O = fmaf(e, o[w * 128 + d], O);
  }
}

__global__ void __launch_bounds__(NTHR, 3)
gqa_decode_kernel(const unsigned short* __restrict__ xin, const unsigned char* __restrict__ kc, const unsigned char* __restrict__ vc,
                  const unsigned char* __restrict__ wq, const unsigned char* __restrict__ wo, unsigned short* __restrict__ out, unsigned char* __restrict__ ws) {
  extern __shared__ __attribute__((aligned(16))) unsigned char smem[];
  const int tid = threadIdx.x, lane = tid & 63;
  const int wave = __builtin_amdgcn_readfirstlane(tid >> 6);
  const int b = blockIdx.x, x = b & 7, j = b >> 3;
  const int r = lane >> 4, c = lane & 15;
  const unsigned lbase = (unsigned)(size_t)(__attribute__((address_space(3))) unsigned char*)smem;
  TS(0);
#define LDS_INIT_BARRIER() do { \
    const int u_ = tid - 4; \
    reinterpret_cast<unsigned*>(smem)[tid] = u_ < 0 ? 0u : (u_ < LA * TPC ? j * HEADB + x * (1u << 20) + (N1 + (unsigned)u_) * TB : NOTREADY); \
    WG_BARRIER(); TS(1); } while (0)
  unsigned tag;

  if (wave == NWS) {
    __builtin_amdgcn_s_setprio(3);
    unsigned char* cntp = ws + WS_CNT + j * 256;
    if (x == 0 && lane == 0) __hip_atomic_store((gu32*)cntp, 0u, RLX_AGENT);
    const unsigned gen = __hip_atomic_load((gu32*)(ws + WS_GEN), RLX_AGENT);
    LDS_INIT_BARRIER();
    tag = __builtin_amdgcn_readfirstlane(gen) + 1u;
    _Pragma("nounroll") for (unsigned spins = 0; spins < SPIN_MAX; ++spins) { if (lds_rd(lbase + L_QCNT) >= 4u) break; }
    {
      const float* qp = reinterpret_cast<const float*>(smem + L_QP);
      const float v0 = (qp[lane] + qp[128 + lane]) + (qp[256 + lane] + qp[384 + lane]);
      const float v1 = (qp[64 + lane] + qp[128 + 64 + lane]) + (qp[256 + 64 + lane] + qp[384 + 64 + lane]);
      unsigned char* qg = ws + WS_QG + ((size_t)(j * 8 + x) * 128 + lane) * 8;
      st_gran(qg, tag, v0); st_gran(qg + 512, tag, v1);
    }
    TS(2);
    {
      const unsigned char* qg = ws + WS_QG + ((size_t)j * 8 * 128 + lane) * 8;
      float q0 = 0.f, q1 = 0.f;
      _Pragma("nounroll") for (unsigned spins = 0; spins < SPIN_MAX; ++spins) {
        unsigned long long g0[8], g1[8];
#pragma unroll
        for (int p = 0; p < 8; ++p) { g0[p] = ld_gran(qg + p * 1024); g1[p] = ld_gran(qg + p * 1024 + 512); }
        bool ok = true; q0 = 0.f; q1 = 0.f;
#pragma unroll
        for (int p = 0; p < 8; ++p) { ok &= tag_ok(g0[p], tag) & tag_ok(g1[p], tag); q0 += gval(g0[p]); q1 += gval(g1[p]); }
        if (__all(ok)) break;
      }
      reinterpret_cast<unsigned short*>(smem + L_Q)[lane] = (unsigned short)f2bf(q0);
      reinterpret_cast<unsigned short*>(smem + L_Q)[lane + 64] = (unsigned short)f2bf(q1);
      asm volatile("s_waitcnt lgkmcnt(0)" ::: "memory");
      if (lane == 0) *(lds_vu32*)(lbase + L_QFLAG) = 1u;
    }
    TS(3);
    __builtin_amdgcn_s_setprio(0);
    if (x == 0) {
      asm volatile("s_waitcnt vmcnt(0)" ::: "memory");
      if (lane == 0) __hip_atomic_store((gu32*)(cntp + 128), tag, RLX_AGENT);
    }
    if (x < 5) {
      rsrc_t wor = __builtin_amdgcn_make_buffer_rsrc((void*)wo, 0, NH * DH * D_MODEL * 2, 0x00020000);
      const unsigned voff = (unsigned)r * (D_MODEL * 2) + (unsigned)c * 16;
      const unsigned soff = (unsigned)(128 * j) * (D_MODEL * 2) + (unsigned)x * 256;
#pragma unroll
      for (int h = 0; h < 2; ++h) {
        u32x4 t[16];
#pragma unroll
        for (int i = 0; i < 16; ++i) t[i] = bld_nt(wor, voff, soff + (unsigned)(h * 16 + i) * 4 * (D_MODEL * 2));
        __builtin_amdgcn_sched_barrier(0);
#pragma unroll
        for (int i = 0; i < 16; ++i) *reinterpret_cast<u32x4*>(smem + L_WO + (h * 16 + i) * 1024 + lane * 16) = t[i];
        __builtin_amdgcn_sched_barrier(0);
      }
    }
    _Pragma("nounroll") for (unsigned spins = 0; spins < SPIN_MAX; ++spins) if (__hip_atomic_load((gu32*)(cntp + 128), RLX_AGENT) == tag) break;
    {
      rsrc_t cr = __builtin_amdgcn_make_buffer_rsrc((void*)cntp, 0, 4, 0x00020000);
      unsigned filled = LA * TPC;
      _Pragma("nounroll") for (;;) {
        _Pragma("nounroll") for (unsigned spins = 0; spins < SPIN_MAX; ++spins) { if (lds_rd(lbase + L_CNT2) + LOOK >= filled) break; __builtin_amdgcn_s_sleep(4); }
        const unsigned g = __builtin_amdgcn_readfirstlane(__builtin_amdgcn_raw_ptr_buffer_atomic_add_i32(1, cr, lane * 4, 0, 0)) + LA * 8;
        if (g < (unsigned)PC && filled + TPC <= (unsigned)(PC * TPC)) {
          if (lane < TPC) *(lds_vu32*)(lbase + L_TAB + 4 * (filled + lane)) = j * HEADB + (g & 7) * (1u << 20) + (N1 + (g >> 3) * TPC + lane) * TB;
          filled += TPC;
        } else {
          _Pragma("nounroll") for (unsigned u = filled + lane; u < (unsigned)MAXU; u += 64) *(lds_vu32*)(lbase + L_TAB + 4 * u) = ENDV;
          break;
        }
      }
    }
    TS(4);
  } else {
    rsrc_t kr = __builtin_amdgcn_make_buffer_rsrc((void*)kc, 0, KVB, 0x00020000);
    rsrc_t vr = __builtin_amdgcn_make_buffer_rsrc((void*)vc, 0, KVB, 0x00020000);
    const unsigned loff = lane * 16;
    const unsigned base1 = j * HEADB + x * (1u << 20);
    u32x4 kb[NPRE][4], vb[NPRE][4];
#define ISSUE_K(S, OFF) do { _Pragma("unroll") for (int i = 0; i < 4; ++i) kb[S][i] = bld_nt(kr, loff + i * 1024, (OFF)); __builtin_amdgcn_sched_barrier(0); } while (0)
#define ISSUE_V(S, OFF) do { _Pragma("unroll") for (int i = 0; i < 4; ++i) vb[S][i] = bld_nt(vr, loff + i * 1024, (OFF)); __builtin_amdgcn_sched_barrier(0); } while (0)
#define ISSUE(S, OFF) do { const unsigned o_ = (OFF); ISSUE_K(S, o_); ISSUE_V(S, o_); } while (0)
    if (wave >= 4) {
      __builtin_amdgcn_s_setprio(3);
      const int row0 = 80 * x + 20 * (wave - 4);
      rsrc_t wqr = __builtin_amdgcn_make_buffer_rsrc((void*)wq, 0, D_MODEL * NH * DH * 2, 0x00020000);
      const unsigned wq_voff = (unsigned)r * (NH * DH * 2) + (unsigned)c * 16;
      const unsigned wq_soff = (unsigned)row0 * (NH * DH * 2) + (unsigned)j * (DH * 2);
      unsigned xv[5]; u32x4 w[5];
#pragma unroll
      for (int i = 0; i < 5; ++i) xv[i] = reinterpret_cast<const unsigned*>(xin)[(row0 + 4 * i + r) >> 1];
#pragma unroll
      for (int i = 0; i < 5; ++i) w[i] = bld_nt(wqr, wq_voff, wq_soff + (unsigned)i * 4 * (NH * DH * 2));
      __builtin_amdgcn_sched_barrier(0);
      LDS_INIT_BARRIER();
      float qa[8];
#pragma unroll
      for (int k = 0; k < 8; ++k) qa[k] = 0.f;
#pragma unroll
      for (int i = 0; i < 5; ++i) {
        const unsigned xl = (r & 1) ? xv[i] >> 16 : xv[i] & 0xffffu, xh = xl << 16;
#pragma unroll
        for (int k = 0; k < 4; ++k) { qa[2 * k] = dot2(w[i][k], xl, qa[2 * k]); qa[2 * k + 1] = dot2(w[i][k], xh, qa[2 * k + 1]); }
      }
#pragma unroll
      for (int k = 0; k < 8; ++k) qa[k] = xrow_sum(qa[k]);
      if (r == 0) {
        float* qp = reinterpret_cast<float*>(smem + L_QP) + (wave - 4) * 128 + 8 * c;
        *reinterpret_cast<f32x4*>(qp) = f32x4{qa[0], qa[1], qa[2], qa[3]};
        *reinterpret_cast<f32x4*>(qp + 4) = f32x4{qa[4], qa[5], qa[6], qa[7]};
      }
      asm volatile("s_waitcnt lgkmcnt(0)" ::: "memory");
      (void)lds_ticket(lbase + L_QCNT);
      TS(12);
      __builtin_amdgcn_s_setprio(X_YPRIO);
      __builtin_amdgcn_sched_barrier(0);
      ISSUE_K(0, base1 + wave * TB);
    } else {
      ISSUE_K(0, base1 + wave * TB);
      LDS_INIT_BARRIER();
    }
    ISSUE_V(0, base1 + wave * TB);
#pragma unroll
    for (int s = 1; s < NPRE; ++s) ISSUE(s, base1 + (s * NWS + wave) * TB);
    const unsigned gen = __hip_atomic_load((gu32*)(ws + WS_GEN), RLX_AGENT);
    __builtin_amdgcn_sched_barrier(0);
    TS(2);
    _Pragma("nounroll") for (unsigned spins = 0; spins < SPIN_MAX; ++spins) { if (lds_rd(lbase + L_QFLAG) != 0u) break; __builtin_amdgcn_s_sleep(1); }
    const u32x4 qv = *reinterpret_cast<const u32x4*>(smem + L_Q + c * 16);
    TS(3);
    constexpr float C2 = 0.08838834764831845f * 1.4426950408889634f;
    float m = -INFINITY, l = 0.f, acc[8];
#pragma unroll
    for (int k = 0; k < 8; ++k) acc[k] = 0.f;
    bool dyn = false;
    auto next = [&]() -> unsigned {
      if (wave >= NWD) return ENDV;
      if (!dyn) {
        const unsigned t = lds_ticket(lbase + L_CNT1);
        if (t < (unsigned)(N1 - NPRE * NWS)) return base1 + (NPRE * NWS + t) * TB;
        dyn = true;
      }
      const unsigned u = lds_ticket(lbase + L_CNT2);
      if (u >= (unsigned)MAXU) return ENDV;
      unsigned v = ENDV;
      _Pragma("nounroll") for (unsigned spins = 0; spins < SPIN_MAX; ++spins) { v = lds_rd(lbase + L_TAB + 4 * u); if (v != NOTREADY) break; __builtin_amdgcn_s_sleep(1); }
      return v == NOTREADY ? ENDV : v;
    };
#define CONSUME(S) do { \
      float s_[4]; \
      _Pragma("unroll") for (int i = 0; i < 4; ++i) { float d_ = dot2(kb[S][i][0], qv[0], 0.f); d_ = dot2(kb[S][i][1], qv[1], d_); d_ = dot2(kb[S][i][2], qv[2], d_); d_ = dot2(kb[S][i][3], qv[3], d_); s_[i] = rowsum16(d_); } \
      const float mn_ = fmaxf(m, fmaxf(fmaxf(s_[0], s_[1]), fmaxf(s_[2], s_[3])) * C2); \
      const float al_ = __builtin_amdgcn_exp2f(m - mn_); m = mn_; l *= al_; \
      _Pragma("unroll") for (int k = 0; k < 8; ++k) acc[k] *= al_; \
      _Pragma("unroll") for (int i = 0; i < 4; ++i) { const float p_ = __builtin_amdgcn_exp2f(fmaf(s_[i], C2, -mn_)); l += p_; \
        _Pragma("unroll") for (int k = 0; k < 4; ++k) { acc[2 * k] = fmaf(p_, bf_lo(vb[S][i][k]), acc[2 * k]); acc[2 * k + 1] = fmaf(p_, bf_hi(vb[S][i][k]), acc[2 * k + 1]); } } \
      __builtin_amdgcn_sched_barrier(0); \
    } while (0)
    bool v0, v1;
    { const unsigned off = next(); CONSUME(0); v0 = off != ENDV; ISSUE(0, off); }
    { const unsigned off = next(); CONSUME(1); v1 = off != ENDV; ISSUE(1, off); }
#pragma unroll
    for (int s = 2; s < NPRE; ++s) CONSUME(s);
    _Pragma("nounroll") for (;;) {
      if (!v0) break;
      { const unsigned off = next(); CONSUME(0); v0 = off != ENDV; ISSUE(0, off); }
      if (!v1) break;
      { const unsigned off = next(); CONSUME(1); v1 = off != ENDV; ISSUE(1, off); }
    }
    TS(4);
    tag = __builtin_amdgcn_readfirstlane(gen) + 1u;
    const float mw = xrow_max(m);
    const float sc = __builtin_amdgcn_exp2f(m - mw);
    const float lw = xrow_sum(l * sc);
#pragma unroll
    for (int k = 0; k < 8; ++k) acc[k] = xrow_sum(acc[k] * sc);
    if (r == 0) {
      float* rec = reinterpret_cast<float*>(smem + L_REC) + wave * 128 + 8 * c;
      *reinterpret_cast<f32x4*>(rec) = f32x4{acc[0], acc[1], acc[2], acc[3]};
      *reinterpret_cast<f32x4*>(rec + 4) = f32x4{acc[4], acc[5], acc[6], acc[7]};
      if (c == 0) { reinterpret_cast<float*>(smem + L_ML)[wave] = mw; reinterpret_cast<float*>(smem + L_ML)[8 + wave] = lw; }
    }
  }
  WG_BARRIER();
  TS(5);
  if (tid < 144) {
    float M, L, O;
    merge8(smem + L_ML, reinterpret_cast<const float*>(smem + L_REC), tid & 127, M, L, O);
    st_gran(ws + WS_PG + (size_t)(j * 8 + x) * 1152 + tid * 8, tag, tid < 128 ? O : (tid == 128 ? M : L));
  }
  if (x >= 5) { TSDUMP(); return; }
  TS(6);
  u32x4 wv[4];
  if (wave < NWS) {
#pragma unroll
    for (int i = 0; i < 4; ++i) wv[i] = *reinterpret_cast<const u32x4*>(smem + L_WO + (wave * 4 + i) * 1024 + lane * 16);
    const unsigned char* pg = ws + WS_PG + (size_t)(j * 8 + wave) * 1152 + lane * 8;
    const unsigned char* pml = ws + WS_PG + (size_t)(j * 8 + wave) * 1152 + 1024 + (lane & 1) * 8;
    unsigned long long g0 = 0, g1 = 0, gm = 0;
    _Pragma("nounroll") for (unsigned spins = 0; spins < SPIN_MAX; ++spins) {
      g0 = ld_gran(pg); g1 = ld_gran(pg + 512); gm = ld_gran(pml);
      if (__all(tag_ok(g0, tag) & tag_ok(g1, tag) & tag_ok(gm, tag))) break;
    }
    float* pgl = reinterpret_cast<float*>(smem + L_PG) + wave * 128;
    pgl[lane] = gval(g0); pgl[64 + lane] = gval(g1);
    if (lane < 2) reinterpret_cast<float*>(smem + L_ML2)[lane * 8 + wave] = gval(gm);
  }
  WG_BARRIER();
  TS(7);
  if (wave < NWS) {
    const f32x4 m0 = *reinterpret_cast<const f32x4*>(smem + L_ML2), m1 = *reinterpret_cast<const f32x4*>(smem + L_ML2 + 16);
    const f32x4 l0 = *reinterpret_cast<const f32x4*>(smem + L_ML2 + 32), l1 = *reinterpret_cast<const f32x4*>(smem + L_ML2 + 48);
    const float* pgl = reinterpret_cast<const float*>(smem + L_PG) + (2 * r) * 128 + wave * 16 + c;
    const float oa = pgl[0], ob = pgl[128];
    const float M = fmaxf(fmaxf(fmaxf(m0[0], m0[1]), fmaxf(m0[2], m0[3])), fmaxf(fmaxf(m1[0], m1[1]), fmaxf(m1[2], m1[3])));
    float e[8], L = 0.f;
#pragma unroll
    for (int w = 0; w < 8; ++w) { e[w] = __builtin_amdgcn_exp2f((w < 4 ? m0[w & 3] : m1[w & 3]) - M); L = fmaf(e[w], w < 4 ? l0[w & 3] : l1[w & 3], L); }
    const bool r1 = (r & 1) != 0, r2 = (r & 2) != 0;
    const float ea = r2 ? (r1 ? e[6] : e[4]) : (r1 ? e[2] : e[0]), eb = r2 ? (r1 ? e[7] : e[5]) : (r1 ? e[3] : e[1]);
    const float o = bf_lo(f2bf(xrow_sum(fmaf(ea, oa, eb * ob)) / L));
    float a[8];
#pragma unroll
    for (int k = 0; k < 8; ++k) a[k] = 0.f;
#pragma unroll
    for (int i = 0; i < 4; ++i) {
      const float ov = __builtin_bit_cast(float, __builtin_amdgcn_ds_bpermute((4 * i + r) * 4, __builtin_bit_cast(int, o)));
#pragma unroll
      for (int k = 0; k < 4; ++k) { a[2 * k] = fmaf(ov, bf_lo(wv[i][k]), a[2 * k]); a[2 * k + 1] = fmaf(ov, bf_hi(wv[i][k]), a[2 * k + 1]); }
    }
#pragma unroll
    for (int k = 0; k < 8; ++k) a[k] = xrow_sum(a[k]);
    if (r == 0) {
      float* yp = reinterpret_cast<float*>(smem + L_YP) + wave * 128 + 8 * c;
      *reinterpret_cast<f32x4*>(yp) = f32x4{a[0], a[1], a[2], a[3]};
      *reinterpret_cast<f32x4*>(yp + 4) = f32x4{a[4], a[5], a[6], a[7]};
    }
  }
  WG_BARRIER();
  TS(8);
  if (tid < 128) {
    const float* yp = reinterpret_cast<const float*>(smem + L_YP);
    float y = 0.f;
#pragma unroll
    for (int w = 0; w < 8; ++w) y += yp[w * 128 + tid];
    st_gran(ws + WS_YG + (((size_t)x * 32 + j) * 128 + tid) * 8, tag, y);
  }
  if (wave == 2) {
    const unsigned char* yg = ws + WS_YG + (((size_t)x * 32 + (lane >> 1)) * 128 + 4 * j + (lane & 1) * 2) * 8;
    unsigned long long g0 = 0, g1 = 0;
    _Pragma("nounroll") for (unsigned spins = 0; spins < SPIN_MAX; ++spins) {
      g0 = ld_gran(yg); g1 = ld_gran(yg + 8);
      if (__all(tag_ok(g0, tag) & tag_ok(g1, tag))) break;
    }
    float y0 = gval(g0), y1 = gval(g1);
    y0 += dpp<0x4E>(y0); y1 += dpp<0x4E>(y1);
    y0 += dpp<0x124>(y0); y1 += dpp<0x124>(y1);
    y0 += dpp<0x128>(y0); y1 += dpp<0x128>(y1);
    y0 = xrow_sum(y0); y1 = xrow_sum(y1);
    if (lane < 2) *reinterpret_cast<unsigned*>(out + 128 * x + 4 * j + 2 * lane) = f2bf(y0) | (f2bf(y1) << 16);
    if (b == 0 && lane == 0) __hip_atomic_store((gu32*)(ws + WS_GEN), tag, RLX_AGENT);
    TS(9);
  }
  TSDUMP();
}

extern "C" void kernel_launch(void* const* d_in, const int* in_sizes, int n_in, void* d_out, int out_size, void* d_ws, size_t ws_size, hipStream_t stream) {
  (void)in_sizes; (void)n_in; (void)out_size; (void)ws_size;
  (void)hipFuncSetAttribute((const void*)gqa_decode_kernel, hipFuncAttributeMaxDynamicSharedMemorySize, LDS_BYTES);
  gqa_decode_kernel<<<256, NTHR, LDS_BYTES, stream>>>((const unsigned short*)d_in[0], (const unsigned char*)d_in[1], (const unsigned char*)d_in[2],
                                                     (const unsigned char*)d_in[3], (const unsigned char*)d_in[4], (unsigned short*)d_out, (unsigned char*)d_ws);
}
```

```cpp
#include <hip/hip_runtime.h>
#include <hip/hip_bf16.h>
#include <stdint.h>

typedef __attribute__((ext_vector_type(4))) unsigned u32x4;
typedef __attribute__((ext_vector_type(4))) float f32x4;
typedef __attribute__((ext_vector_type(2))) float f32x2;
typedef __attribute__((ext_vector_type(2))) __bf16 bf16x2;
typedef __amdgpu_buffer_rsrc_t rsrc_t;
typedef __attribute__((address_space(1))) unsigned long long gu64;
typedef __attribute__((address_space(1))) unsigned gu32;
typedef __attribute__((address_space(3))) volatile unsigned lds_vu32;

#define DBG_TS 0

constexpr int D_MODEL = 640, NH = 32, SK = 32768, DH = 128;
constexpr int NWS = 8;
#define NWD 4
constexpr int NTHR = (NWS + 1) * 64;
constexpr unsigned TB = 4096;
constexpr int TPB = 256;
#define N1 192
#define TPC 2
#define LA 5
#define LOOK 8
#define KMAX 4u
#define POLL_STAGGER 8
#define NPRE 3
constexpr int CPB = (TPB - N1) / TPC;
constexpr int PC = 8 * CPB;
constexpr int MAXU = PC * TPC + 32;
constexpr unsigned HEADB = 8u << 20, KVB = 256u << 20;
constexpr unsigned NOTREADY = 0xffffffffu, ENDV = 0x80000000u;
constexpr unsigned SPIN_MAX = 1u << 16;

constexpr size_t WS_GEN = 0;
constexpr size_t WS_CNT = 4096;
constexpr size_t WS_QG = 16384;
constexpr size_t WS_PG = WS_QG + 32 * 8 * 1024;
constexpr size_t WS_YG = WS_PG + 32 * 8 * 1152;
constexpr size_t WS_DBG = 1u << 20;

constexpr int L_CNT1 = 0, L_CNT2 = 4, L_QFLAG = 8, L_QCNT = 12;
constexpr int L_TAB = 16;
constexpr int L_Q = NTHR * 4;
constexpr int L_ML = L_Q + 256;
constexpr int L_ML2 = L_ML + 64;
constexpr int L_REC = L_ML2 + 64;
constexpr int L_PG = L_REC + 8 * 128 * 4;
constexpr int L_YP = L_PG + 8 * 128 * 4;
constexpr int L_QP = L_YP + 8 * 128 * 4;
constexpr int L_WO = L_QP + 4 * 128 * 4;
constexpr int L_END = L_WO + 32768;
constexpr int LDS_BYTES = 84 * 1024;
static_assert(L_END <= LDS_BYTES, "lds");
static_assert(MAXU + 4 <= NTHR, "table init");

#define RLX_AGENT __ATOMIC_RELAXED, __HIP_MEMORY_SCOPE_AGENT

__device__ __forceinline__ float dot2(unsigned a, unsigned b, float acc) {
  return __builtin_amdgcn_fdot2_f32_bf16(__builtin_bit_cast(bf16x2, a), __builtin_bit_cast(bf16x2, b), acc, false);
}
template <int CTRL> __device__ __forceinline__ float dpp(float x) {
  return __builtin_bit_cast(float, __builtin_amdgcn_mov_dpp(__builtin_bit_cast(int, x), CTRL, 0xf, 0xf, true));
}
__device__ __forceinline__ float rowsum16(float x) {
  x += dpp<0x128>(x); x += dpp<0x141>(x); x += dpp<0x4E>(x); x += dpp<0xB1>(x); return x;
}
__device__ __forceinline__ float xrow_sum(float x) {
  auto s = __builtin_amdgcn_permlane16_swap(__float_as_uint(x), __float_as_uint(x), false, false);
  x = __uint_as_float(s[0]) + __uint_as_float(s[1]);
  auto t = __builtin_amdgcn_permlane32_swap(__float_as_uint(x), __float_as_uint(x), false, false);
  return __uint_as_float(t[0]) + __uint_as_float(t[1]);
}
__device__ __forceinline__ float xrow_max(float x) {
  auto s = __builtin_amdgcn_permlane16_swap(__float_as_uint(x), __float_as_uint(x), false, false);
  x = fmaxf(__uint_as_float(s[0]), __uint_as_float(s[1]));
  auto t = __builtin_amdgcn_permlane32_swap(__float_as_uint(x), __float_as_uint(x), false, false);
  return fmaxf(__uint_as_float(t[0]), __uint_as_float(t[1]));
}
__device__ __forceinline__ float swap16_add(float a, float b) {
  auto s = __builtin_amdgcn_permlane16_swap(__float_as_uint(a), __float_as_uint(b), false, false);
  return __uint_as_float(s[0]) + __uint_as_float(s[1]);
}
__device__ __forceinline__ float swap32_add(float a, float b) {
  auto s = __builtin_amdgcn_permlane32_swap(__float_as_uint(a), __float_as_uint(b), false, false);
  return __uint_as_float(s[0]) + __uint_as_float(s[1]);
}
__device__ __forceinline__ f32x2 treduce8(const float (&t)[8]) {
  f32x2 o;
  o[0] = swap32_add(swap16_add(t[0], t[2]), swap16_add(t[4], t[6]));
  o[1] = swap32_add(swap16_add(t[1], t[3]), swap16_add(t[5], t[7]));
  return o;
}
__device__ __forceinline__ float bf_lo(unsigned u) { return __builtin_bit_cast(float, u << 16); }
__device__ __forceinline__ float bf_hi(unsigned u) { return __builtin_bit_cast(float, u & 0xffff0000u); }
__device__ __forceinline__ unsigned f2bf(float f) { __bf16 b = (__bf16)f; return (unsigned)__builtin_bit_cast(unsigned short, b); }
__device__ __forceinline__ u32x4 bld_nt(rsrc_t r, unsigned voff, unsigned soff) { return __builtin_amdgcn_raw_buffer_load_b128(r, voff, soff, 2); }
__device__ __forceinline__ void st_gran(unsigned char* p, unsigned tag, float v) {
  __hip_atomic_store((gu64*)p, ((unsigned long long)tag << 32) | __float_as_uint(v), RLX_AGENT);
}
__device__ __forceinline__ unsigned long long ld_gran(const unsigned char* p) { return __hip_atomic_load((gu64*)p, RLX_AGENT); }
__device__ __forceinline__ bool tag_ok(unsigned long long g, unsigned tag) { return (unsigned)(g >> 32) == tag; }
__device__ __forceinline__ float gval(unsigned long long g) { return __uint_as_float((unsigned)g); }

#define POLL_CHECK(P) \
    "v_lshrrev_b64 %[t], 32, %[" #P "0]\n\tv_cmp_eq_u64 vcc, %[tg], %[t]\n\t" \
    "v_lshrrev_b64 %[t], 32, %[" #P "1]\n\tv_cmp_eq_u64 %[m], %[tg], %[t]\n\ts_and_b64 vcc, vcc, %[m]\n\t"
#define POLL_CHECK_X(P) "v_lshrrev_b64 %[t], 32, %[" #P "2]\n\tv_cmp_eq_u64 %[m], %[tg], %[t]\n\ts_and_b64 vcc, vcc, %[m]\n\t"
#define STR2(x) #x
#define STR(x) STR2(x)
__device__ __forceinline__ unsigned poll3(unsigned long long& a0, unsigned long long& a1, unsigned long long& a2,
                                          unsigned long long& b0, unsigned long long& b1, unsigned long long& b2,
                                          const unsigned char* p, const unsigned char* p2, unsigned tag) {
  unsigned which, cnt; unsigned long long t, m; const unsigned long long tg = tag;
  asm volatile(
    "s_waitcnt vmcnt(0)\n\t"
    "global_load_dwordx2 %[a0], %[p], off sc1\n\tglobal_load_dwordx2 %[a1], %[p], off offset:512 sc1\n\tglobal_load_dwordx2 %[a2], %[p2], off sc1\n\t"
    "s_sleep " STR(POLL_STAGGER) "\n\t"
    "global_load_dwordx2 %[b0], %[p], off sc1\n\tglobal_load_dwordx2 %[b1], %[p], off offset:512 sc1\n\tglobal_load_dwordx2 %[b2], %[p2], off sc1\n\t"
    "s_mov_b32 %[cnt], 0x10000\n"
    "1:\n\t"
    "s_mov_b32 %[w], 0\n\ts_waitcnt vmcnt(3)\n\t"
    POLL_CHECK(a) POLL_CHECK_X(a)
    "s_cmp_eq_u64 vcc, exec\n\ts_cbranch_scc1 2f\n\t"
    "global_load_dwordx2 %[a0], %[p], off sc1\n\tglobal_load_dwordx2 %[a1], %[p], off offset:512 sc1\n\tglobal_load_dwordx2 %[a2], %[p2], off sc1\n\t"
    "s_mov_b32 %[w], 1\n\ts_waitcnt vmcnt(3)\n\t"
    POLL_CHECK(b) POLL_CHECK_X(b)
    "s_cmp_eq_u64 vcc, exec\n\ts_cbranch_scc1 2f\n\t"
    "global_load_dwordx2 %[b0], %[p], off sc1\n\tglobal_load_dwordx2 %[b1], %[p], off offset:512 sc1\n\tglobal_load_dwordx2 %[b2], %[p2], off sc1\n\t"
    "s_sub_u32 %[cnt], %[cnt], 1\n\ts_cmp_lg_u32 %[cnt], 0\n\ts_cbranch_scc1 1b\n"
    "2:"
    : [a0] "=&v"(a0), [a1] "=&v"(a1), [a2] "=&v"(a2), [b0] "=&v"(b0), [b1] "=&v"(b1), [b2] "=&v"(b2), [w] "=&s"(which), [cnt] "=&s"(cnt), [t] "=&v"(t), [m] "=&s"(m)
    : [p] "v"(p), [p2] "v"(p2), [tg] "s"(tg) : "vcc", "scc", "memory");
  return which;
}
__device__ __forceinline__ unsigned poll2(unsigned long long& a0, unsigned long long& a1, unsigned long long& b0, unsigned long long& b1,
                                          const unsigned char* p, unsigned tag) {
  unsigned which, cnt; unsigned long long t, m; const unsigned long long tg = tag;
  asm volatile(
    "s_waitcnt vmcnt(0)\n\t"
    "global_load_dwordx2 %[a0], %[p], off sc1\n\tglobal_load_dwordx2 %[a1], %[p], off offset:8 sc1\n\t"
    "s_sleep " STR(POLL_STAGGER) "\n\t"
    "global_load_dwordx2 %[b0], %[p], off sc1\n\tglobal_load_dwordx2 %[b1], %[p], off offset:8 sc1\n\t"
    "s_mov_b32 %[cnt], 0x10000\n"
    "1:\n\t"
    "s_mov_b32 %[w], 0\n\ts_waitcnt vmcnt(2)\n\t"
    POLL_CHECK(a)
    "s_cmp_eq_u64 vcc, exec\n\ts_cbranch_scc1 2f\n\t"
    "global_load_dwordx2 %[a0], %[p], off sc1\n\tglobal_load_dwordx2 %[a1], %[p], off offset:8 sc1\n\t"
    "s_mov_b32 %[w], 1\n\ts_waitcnt vmcnt(2)\n\t"
    POLL_CHECK(b)
    "s_cmp_eq_u64 vcc, exec\n\ts_cbranch_scc1 2f\n\t"
    "global_load_dwordx2 %[b0], %[p], off sc1\n\tglobal_load_dwordx2 %[b1], %[p], off offset:8 sc1\n\t"
    "s_sub_u32 %[cnt], %[cnt], 1\n\ts_cmp_lg_u32 %[cnt], 0\n\ts_cbranch_scc1 1b\n"
    "2:"
    : [a0] "=&v"(a0), [a1] "=&v"(a1), [b0] "=&v"(b0), [b1] "=&v"(b1), [w] "=&s"(which), [cnt] "=&s"(cnt), [t] "=&v"(t), [m] "=&s"(m)
    : [p] "v"(p), [tg] "s"(tg) : "vcc", "scc", "memory");
  return which;
}
#define POLL_DRAIN3(a0, a1, a2, b0, b1, b2) asm volatile("s_waitcnt vmcnt(0)" : "+v"(a0), "+v"(a1), "+v"(a2), "+v"(b0), "+v"(b1), "+v"(b2) :: "memory")
#define POLL_DRAIN2(a0, a1, b0, b1) asm volatile("s_waitcnt vmcnt(0)" : "+v"(a0), "+v"(a1), "+v"(b0), "+v"(b1) :: "memory")
__device__ __forceinline__ unsigned lds_ticket(unsigned addr) {
  unsigned r, t0, t1;
  asm volatile("v_mov_b32 %1, %3\n\tv_mov_b32 %2, 1\n\ts_mov_b64 exec, 1\n\tds_add_rtn_u32 %1, %1, %2\n\ts_waitcnt lgkmcnt(0)\n\ts_mov_b64 exec, -1\n\tv_readfirstlane_b32 %0, %1"
               : "=s"(r), "=&v"(t0), "=&v"(t1) : "s"(addr) : "memory");
  return r;
}
__device__ __forceinline__ unsigned lds_rd(unsigned addr) {
  unsigned r, t0;
  asm volatile("v_mov_b32 %1, %2\n\tds_read_b32 %1, %1\n\ts_waitcnt lgkmcnt(0)\n\tv_readfirstlane_b32 %0, %1" : "=s"(r), "=&v"(t0) : "s"(addr) : "memory");
  return r;
}
#define WG_BARRIER() do { __builtin_amdgcn_sched_barrier(0); asm volatile("s_waitcnt lgkmcnt(0)\n\ts_barrier" ::: "memory"); __builtin_amdgcn_sched_barrier(0); } while (0)

#define TS(slot) do {} while (0)
#define TSDUMP() do {} while (0)

__device__ __forceinline__ void merge8(const unsigned char* ml, const float* o, int d, float& M, float& L, float& O) {
  const f32x4 m0 = *reinterpret_cast<const f32x4*>(ml), m1 = *reinterpret_cast<const f32x4*>(ml + 16);
  const f32x4 l0 = *reinterpret_cast<const f32x4*>(ml + 32), l1 = *reinterpret_cast<const f32x4*>(ml + 48);
  M = fmaxf(fmaxf(fmaxf(m0[0], m0[1]), fmaxf(m0[2], m0[3])), fmaxf(fmaxf(m1[0], m1[1]), fmaxf(m1[2], m1[3])));
  L = 0.f; O = 0.f;
#pragma unroll
  for (int w = 0; w < 8; ++w) {
    const float e = __builtin_amdgcn_exp2f((w < 4 ? m0[w & 3] : m1[w & 3]) - M);
    L = fmaf(e, w < 4 ? l0[w & 3] : l1[w & 3], L);
    O = fmaf(e, o[w * 128 + d], O);
  }
}

__global__ void __launch_bounds__(NTHR, 3)
gqa_decode_kernel(const unsigned short* __restrict__ xin, const unsigned char* __restrict__ kc, const unsigned char* __restrict__ vc,
                  const unsigned char* __restrict__ wq, const unsigned char* __restrict__ wo, unsigned short* __restrict__ out, unsigned char* __restrict__ ws) {
  extern __shared__ __attribute__((aligned(16))) unsigned char smem[];
  const int tid = threadIdx.x, lane = tid & 63;
  const int wave = __builtin_amdgcn_readfirstlane(tid >> 6);
  const int b = blockIdx.x, x = b & 7, j = b >> 3;
  const int r = lane >> 4, c = lane & 15;
  const unsigned lbase = (unsigned)(size_t)(__attribute__((address_space(3))) unsigned char*)smem;
  TS(0);
#define LDS_INIT_BARRIER() do { \
    const int u_ = tid - 4; \
    reinterpret_cast<unsigned*>(smem)[tid] = u_ < 0 ? 0u : (u_ < LA * TPC ? j * HEADB + x * (1u << 20) + (N1 + (unsigned)u_) * TB : NOTREADY); \
    WG_BARRIER(); TS(1); } while (0)
  unsigned tag;

  if (wave == NWS) {
    __builtin_amdgcn_s_setprio(3);
    unsigned char* cntp = ws + WS_CNT + j * 256;
    if (x == 0 && lane == 0) __hip_atomic_store((gu32*)cntp, 0u, RLX_AGENT);
    const unsigned gen = __hip_atomic_load((gu32*)(ws + WS_GEN), RLX_AGENT);
    LDS_INIT_BARRIER();
    tag = __builtin_amdgcn_readfirstlane(gen) + 1u;
    _Pragma("nounroll") for (unsigned spins = 0; spins < SPIN_MAX; ++spins) { if (lds_rd(lbase + L_QCNT) >= 4u) break; }
    {
      const float* qp = reinterpret_cast<const float*>(smem + L_QP);
      const float v0 = (qp[lane] + qp[128 + lane]) + (qp[256 + lane] + qp[384 + lane]);
      const float v1 = (qp[64 + lane] + qp[128 + 64 + lane]) + (qp[256 + 64 + lane] + qp[384 + 64 + lane]);
      unsigned char* qg = ws + WS_QG + ((size_t)(j * 8 + x) * 128 + lane) * 8;
      st_gran(qg, tag, v0); st_gran(qg + 512, tag, v1);
    }
    TS(2);
    {
      const unsigned char* qg = ws + WS_QG + ((size_t)j * 8 * 128 + lane) * 8;
      float q0 = 0.f, q1 = 0.f;
      _Pragma("nounroll") for (unsigned spins = 0; spins < SPIN_MAX; ++spins) {
        unsigned long long g0[8], g1[8];
#pragma unroll
        for (int p = 0; p < 8; ++p) { g0[p] = ld_gran(qg + p * 1024); g1[p] = ld_gran(qg + p * 1024 + 512); }
        bool ok = true; q0 = 0.f; q1 = 0.f;
#pragma unroll
        for (int p = 0; p < 8; ++p) { ok &= tag_ok(g0[p], tag) & tag_ok(g1[p], tag); q0 += gval(g0[p]); q1 += gval(g1[p]); }
        if (__all(ok)) break;
      }
      reinterpret_cast<unsigned short*>(smem + L_Q)[lane] = (unsigned short)f2bf(q0);
      reinterpret_cast<unsigned short*>(smem + L_Q)[lane + 64] = (unsigned short)f2bf(q1);
      asm volatile("s_waitcnt lgkmcnt(0)" ::: "memory");
      if (lane == 0) *(lds_vu32*)(lbase + L_QFLAG) = 1u;
    }
    TS(3);
    __builtin_amdgcn_s_setprio(0);
    if (x == 0) {
      asm volatile("s_waitcnt vmcnt(0)" ::: "memory");
      if (lane == 0) __hip_atomic_store((gu32*)(cntp + 128), tag, RLX_AGENT);
    }
    if (x < 5) {
      rsrc_t wor = __builtin_amdgcn_make_buffer_rsrc((void*)wo, 0, NH * DH * D_MODEL * 2, 0x00020000);
      const unsigned voff = (unsigned)r * (D_MODEL * 2) + (unsigned)c * 16;
      const unsigned soff = (unsigned)(128 * j) * (D_MODEL * 2) + (unsigned)x * 256;
#pragma unroll
      for (int h = 0; h < 2; ++h) {
        u32x4 t[16];
#pragma unroll
        for (int i = 0; i < 16; ++i) t[i] = bld_nt(wor, voff, soff + (unsigned)(h * 16 + i) * 4 * (D_MODEL * 2));
        __builtin_amdgcn_sched_barrier(0);
#pragma unroll
        for (int i = 0; i < 16; ++i) *reinterpret_cast<u32x4*>(smem + L_WO + (h * 16 + i) * 1024 + lane * 16) = t[i];
        __builtin_amdgcn_sched_barrier(0);
      }
    }
    _Pragma("nounroll") for (unsigned spins = 0; spins < SPIN_MAX; ++spins) if (__hip_atomic_load((gu32*)(cntp + 128), RLX_AGENT) == tag) break;
    {
      rsrc_t cr = __builtin_amdgcn_make_buffer_rsrc((void*)cntp, 0, 4, 0x00020000);
      unsigned filled = LA * TPC;
      _Pragma("nounroll") for (;;) {
        unsigned taken = 0;
        _Pragma("nounroll") for (unsigned spins = 0; spins < SPIN_MAX; ++spins) { taken = lds_rd(lbase + L_CNT2); if (taken + LOOK >= filled) break; __builtin_amdgcn_s_sleep(2); }
        unsigned k = (taken + LOOK - filled) / TPC + 1u; k = k > KMAX ? KMAX : k;
        const unsigned gv = (unsigned)__builtin_amdgcn_raw_ptr_buffer_atomic_add_i32(1, cr, (unsigned)lane < k ? 0u : 64u, 0, 0) + LA * 8;
        const unsigned g = (unsigned)__builtin_amdgcn_ds_bpermute((lane / TPC) * 4, (int)gv);
        const bool act = (unsigned)lane < k * TPC;
        const unsigned u = filled + lane;
        const bool good = g < (unsigned)PC && u < (unsigned)(PC * TPC);
        if (act) *(lds_vu32*)(lbase + L_TAB + 4 * u) = good ? j * HEADB + (g & 7) * (1u << 20) + (N1 + (g >> 3) * TPC + (lane % TPC)) * TB : ENDV;
        filled += k * TPC;
        if (__any(act && !good)) {
          _Pragma("nounroll") for (unsigned u2 = filled + lane; u2 < (unsigned)MAXU; u2 += 64) *(lds_vu32*)(lbase + L_TAB + 4 * u2) = ENDV;
          break;
        }
      }
    }
    TS(4);
  } else {
    rsrc_t kr = __builtin_amdgcn_make_buffer_rsrc((void*)kc, 0, KVB, 0x00020000);
    rsrc_t vr = __builtin_amdgcn_make_buffer_rsrc((void*)vc, 0, KVB, 0x00020000);
    const unsigned loff = lane * 16;
    const unsigned base1 = j * HEADB + x * (1u << 20);
    u32x4 kb[NPRE][4], vb[NPRE][4];
#define ISSUE_K(S, OFF) do { _Pragma("unroll") for (int i = 0; i < 4; ++i) kb[S][i] = bld_nt(kr, loff + i * 1024, (OFF)); __builtin_amdgcn_sched_barrier(0); } while (0)
#define ISSUE_V(S, OFF) do { _Pragma("unroll") for (int i = 0; i < 4; ++i) vb[S][i] = bld_nt(vr, loff + i * 1024, (OFF)); __builtin_amdgcn_sched_barrier(0); } while (0)
#define ISSUE(S, OFF) do { const unsigned o_ = (OFF); ISSUE_K(S, o_); ISSUE_V(S, o_); } while (0)
    if (wave >= 4) {
      __builtin_amdgcn_s_setprio(3);
      const int row0 = 80 * x + 20 * (wave - 4);
      rsrc_t wqr = __builtin_amdgcn_make_buffer_rsrc((void*)wq, 0, D_MODEL * NH * DH * 2, 0x00020000);
      const unsigned wq_voff = (unsigned)r * (NH * DH * 2) + (unsigned)c * 16;
      const unsigned wq_soff = (unsigned)row0 * (NH * DH * 2) + (unsigned)j * (DH * 2);
      unsigned xv[5]; u32x4 w[5];
#pragma unroll
      for (int i = 0; i < 5; ++i) xv[i] = reinterpret_cast<const unsigned*>(xin)[(row0 + 4 * i + r) >> 1];
#pragma unroll
      for (int i = 0; i < 5; ++i) w[i] = bld_nt(wqr, wq_voff, wq_soff + (unsigned)i * 4 * (NH * DH * 2));
      __builtin_amdgcn_sched_barrier(0);
      LDS_INIT_BARRIER();
      float qa[8];
#pragma unroll
      for (int k = 0; k < 8; ++k) qa[k] = 0.f;
#pragma unroll
      for (int i = 0; i < 5; ++i) {
        const unsigned xl = (r & 1) ? xv[i] >> 16 : xv[i] & 0xffffu, xh = xl << 16;
#pragma unroll
        for (int k = 0; k < 4; ++k) { qa[2 * k] = dot2(w[i][k], xl, qa[2 * k]); qa[2 * k + 1] = dot2(w[i][k], xh, qa[2 * k + 1]); }
      }
      *reinterpret_cast<f32x2*>(smem + L_QP + ((wave - 4) * 128 + 8 * c + 2 * r) * 4) = treduce8(qa);
      asm volatile("s_waitcnt lgkmcnt(0)" ::: "memory");
      (void)lds_ticket(lbase + L_QCNT);
      TS(12);
      __builtin_amdgcn_s_setprio(1);
      __builtin_amdgcn_sched_barrier(0);
      ISSUE_K(0, base1 + wave * TB);
    } else {
      ISSUE_K(0, base1 + wave * TB);
      LDS_INIT_BARRIER();
    }
    ISSUE_V(0, base1 + wave * TB);
#pragma unroll
    for (int s = 1; s < NPRE; ++s) ISSUE(s, base1 + (s * NWS + wave) * TB);
    const unsigned gen = __hip_atomic_load((gu32*)(ws + WS_GEN), RLX_AGENT);
    __builtin_amdgcn_sched_barrier(0);
    TS(2);
    _Pragma("nounroll") for (unsigned spins = 0; spins < SPIN_MAX; ++spins) { if (lds_rd(lbase + L_QFLAG) != 0u) break; __builtin_amdgcn_s_sleep(1); }
    const u32x4 qv = *reinterpret_cast<const u32x4*>(smem + L_Q + c * 16);
    TS(3);
    constexpr float C2 = 0.08838834764831845f * 1.4426950408889634f;
    float m = -INFINITY, l = 0.f, acc[8];
#pragma unroll
    for (int k = 0; k < 8; ++k) acc[k] = 0.f;
    bool dyn = false;
    auto next = [&]() -> unsigned {
      if (wave >= NWD) return ENDV;
      if (!dyn) {
        const unsigned t = lds_ticket(lbase + L_CNT1);
        if (t < (unsigned)(N1 - NPRE * NWS)) return base1 + (NPRE * NWS + t) * TB;
        dyn = true;
      }
      const unsigned u = lds_ticket(lbase + L_CNT2);
      if (u >= (unsigned)MAXU) return ENDV;
      unsigned v = ENDV;
      _Pragma("nounroll") for (unsigned spins = 0; spins < SPIN_MAX; ++spins) { v = lds_rd(lbase + L_TAB + 4 * u); if (v != NOTREADY) break; __builtin_amdgcn_s_sleep(1); }
      return v == NOTREADY ? ENDV : v;
    };
#define CONSUME(S) do { \
      float s_[4]; \
      _Pragma("unroll") for (int i = 0; i < 4; ++i) { float d_ = dot2(kb[S][i][0], qv[0], 0.f); d_ = dot2(kb[S][i][1], qv[1], d_); d_ = dot2(kb[S][i][2], qv[2], d_); d_ = dot2(kb[S][i][3], qv[3], d_); s_[i] = rowsum16(d_); } \
      const float mn_ = fmaxf(m, fmaxf(fmaxf(s_[0], s_[1]), fmaxf(s_[2], s_[3])) * C2); \
      const float al_ = __builtin_amdgcn_exp2f(m - mn_); m = mn_; l *= al_; \
      _Pragma("unroll") for (int k = 0; k < 8; ++k) acc[k] *= al_; \
      _Pragma("unroll") for (int i = 0; i < 4; ++i) { const float p_ = __builtin_amdgcn_exp2f(fmaf(s_[i], C2, -mn_)); l += p_; \
        _Pragma("unroll") for (int k = 0; k < 4; ++k) { acc[2 * k] = fmaf(p_, bf_lo(vb[S][i][k]), acc[2 * k]); acc[2 * k + 1] = fmaf(p_, bf_hi(vb[S][i][k]), acc[2 * k + 1]); } } \
      __builtin_amdgcn_sched_barrier(0); \
    } while (0)
    bool v0, v1;
    { const unsigned off = next(); CONSUME(0); v0 = off != ENDV; ISSUE(0, off); }
    { const unsigned off = next(); CONSUME(1); v1 = off != ENDV; ISSUE(1, off); }
#pragma unroll
    for (int s = 2; s < NPRE; ++s) CONSUME(s);
    _Pragma("nounroll") for (;;) {
      if (!v0) break;
      { const unsigned off = next(); CONSUME(0); v0 = off != ENDV; ISSUE(0, off); }
      if (!v1) break;
      { const unsigned off = next(); CONSUME(1); v1 = off != ENDV; ISSUE(1, off); }
    }
    TS(4);
    tag = __builtin_amdgcn_readfirstlane(gen) + 1u;
    const float mw = xrow_max(m);
    const float sc = __builtin_amdgcn_exp2f(m - mw);
    const float lw = xrow_sum(l * sc);
#pragma unroll
    for (int k = 0; k < 8; ++k) acc[k] *= sc;
    *reinterpret_cast<f32x2*>(smem + L_REC + (wave * 128 + 8 * c + 2 * r) * 4) = treduce8(acc);
    if (lane < 2) reinterpret_cast<float*>(smem + L_ML)[lane * 8 + wave] = lane == 0 ? mw : lw;
  }
  WG_BARRIER();
  TS(5);
  if (tid < 144) {
    float M, L, O;
    merge8(smem + L_ML, reinterpret_cast<const float*>(smem + L_REC), tid & 127, M, L, O);
    st_gran(ws + WS_PG + (size_t)(j * 8 + x) * 1152 + tid * 8, tag, tid < 128 ? O : (tid == 128 ? M : L));
  }
  if (x >= 5) { TSDUMP(); return; }
  TS(6);
  u32x4 wv[4];
  unsigned long long pa0 = 0, pa1 = 0, pa2 = 0, pb0 = 0, pb1 = 0, pb2 = 0;
  if (wave < NWS) {
#pragma unroll
    for (int i = 0; i < 4; ++i) wv[i] = *reinterpret_cast<const u32x4*>(smem + L_WO + (wave * 4 + i) * 1024 + lane * 16);
    const unsigned char* pg = ws + WS_PG + (size_t)(j * 8 + wave) * 1152 + lane * 8;
    const unsigned char* pml = ws + WS_PG + (size_t)(j * 8 + wave) * 1152 + 1024 + (lane & 1) * 8;
    const unsigned which = poll3(pa0, pa1, pa2, pb0, pb1, pb2, pg, pml, tag);
    const unsigned long long g0 = which ? pb0 : pa0, g1 = which ? pb1 : pa1, gm = which ? pb2 : pa2;
    float* pgl = reinterpret_cast<float*>(smem + L_PG) + wave * 128;
    pgl[lane] = gval(g0); pgl[64 + lane] = gval(g1);
    if (lane < 2) reinterpret_cast<float*>(smem + L_ML2)[lane * 8 + wave] = gval(gm);
  }
  WG_BARRIER();
  TS(7);
  if (wave < NWS) {
    const f32x2 mm = *reinterpret_cast<const f32x2*>(smem + L_ML2 + r * 8), ll = *reinterpret_cast<const f32x2*>(smem + L_ML2 + 32 + r * 8);
    const float* pgl = reinterpret_cast<const float*>(smem + L_PG) + (2 * r) * 128 + wave * 16 + c;
    const float oa = pgl[0], ob = pgl[128];
    const float M = xrow_max(fmaxf(mm[0], mm[1]));
    const float ea = __builtin_amdgcn_exp2f(mm[0] - M), eb = __builtin_amdgcn_exp2f(mm[1] - M);
    const float num = xrow_sum(fmaf(ea, oa, eb * ob)), den = xrow_sum(fmaf(ea, ll[0], eb * ll[1]));
    const unsigned obf = f2bf(num / den);
    float a[8];
#pragma unroll
    for (int k = 0; k < 8; ++k) a[k] = 0.f;
#pragma unroll
    for (int i = 0; i < 4; ++i) {
      const unsigned ol = (unsigned)__builtin_amdgcn_ds_bpermute((4 * i + r) * 4, (int)obf), oh = ol << 16;
#pragma unroll
      for (int k = 0; k < 4; ++k) { a[2 * k] = dot2(wv[i][k], ol, a[2 * k]); a[2 * k + 1] = dot2(wv[i][k], oh, a[2 * k + 1]); }
    }
    *reinterpret_cast<f32x2*>(smem + L_YP + (wave * 128 + 8 * c + 2 * r) * 4) = treduce8(a);
    POLL_DRAIN3(pa0, pa1, pa2, pb0, pb1, pb2);
  }
  WG_BARRIER();
  TS(8);
  if (tid < 128) {
    const float* yp = reinterpret_cast<const float*>(smem + L_YP);
    float y = 0.f;
#pragma unroll
    for (int w = 0; w < 8; ++w) y += yp[w * 128 + tid];
    st_gran(ws + WS_YG + (((size_t)x * 32 + j) * 128 + tid) * 8, tag, y);
    TS(10);
  }
  if (wave == 2) {
    const unsigned char* yg = ws + WS_YG + (((size_t)x * 32 + (lane >> 1)) * 128 + 4 * j + (lane & 1) * 2) * 8;
    unsigned long long a0, a1, b0, b1;
    const unsigned which = poll2(a0, a1, b0, b1, yg, tag);
    const unsigned long long g0 = which ? b0 : a0, g1 = which ? b1 : a1;
    float y0 = gval(g0), y1 = gval(g1);
    y0 += dpp<0x4E>(y0); y1 += dpp<0x4E>(y1);
    y0 += dpp<0x124>(y0); y1 += dpp<0x124>(y1);
    y0 += dpp<0x128>(y0); y1 += dpp<0x128>(y1);
    y0 = xrow_sum(y0); y1 = xrow_sum(y1);
    if (lane < 2) *reinterpret_cast<unsigned*>(out + 128 * x + 4 * j + 2 * lane) = f2bf(y0) | (f2bf(y1) << 16);
    if (b == 0 && lane == 0) __hip_atomic_store((gu32*)(ws + WS_GEN), tag, RLX_AGENT);
    TS(9);
    POLL_DRAIN2(a0, a1, b0, b1);
  }
  TSDUMP();
}

extern "C" void kernel_launch(void* const* d_in, const int* in_sizes, int n_in, void* d_out, int out_size, void* d_ws, size_t ws_size, hipStream_t stream) {
  (void)in_sizes; (void)n_in; (void)out_size; (void)ws_size;
  (void)hipFuncSetAttribute((const void*)gqa_decode_kernel, hipFuncAttributeMaxDynamicSharedMemorySize, LDS_BYTES);
  gqa_decode_kernel<<<256, NTHR, LDS_BYTES, stream>>>((const unsigned short*)d_in[0], (const unsigned char*)d_in[1], (const unsigned char*)d_in[2],
                                                     (const unsigned char*)d_in[3], (const unsigned char*)d_in[4], (unsigned short*)d_out, (unsigned char*)d_ws);
}
```

```cpp
#include <hip/hip_runtime.h>
#include <hip/hip_bf16.h>
#include <stdint.h>

typedef __attribute__((ext_vector_type(4))) unsigned u32x4;
typedef __attribute__((ext_vector_type(4))) float f32x4;
typedef __attribute__((ext_vector_type(2))) float f32x2;
typedef __attribute__((ext_vector_type(2))) __bf16 bf16x2;
typedef __amdgpu_buffer_rsrc_t rsrc_t;
typedef __attribute__((address_space(1))) unsigned long long gu64;
typedef __attribute__((address_space(1))) unsigned gu32;
typedef __attribute__((address_space(3))) volatile unsigned lds_vu32;
typedef __attribute__((address_space(3))) unsigned char lds_u8;

constexpr int D_MODEL = 640, NH = 32, DH = 128;
constexpr int NWS = 8;
constexpr int NWD = 4;
constexpr int NTHR = (NWS + 1) * 64;
constexpr unsigned TB = 4096;
constexpr int TPB = 256;
constexpr int N1 = 176;
constexpr int TPC = 2;
constexpr int LA = 5;
constexpr unsigned LOOK = 8;
constexpr unsigned KMAX = 4;
constexpr int NPRE = 3;
constexpr int CPB = (TPB - N1) / TPC;
constexpr int PC = 8 * CPB;
constexpr int TABN = PC * TPC < 528 ? PC * TPC : 528;
constexpr int MAXU = TABN + 32;
constexpr unsigned HEADB = 8u << 20, KVB = 256u << 20;
constexpr unsigned NOTREADY = 0xffffffffu, ENDV = 0x80000000u;
constexpr unsigned SPIN_G = 1u << 17;
constexpr unsigned SPIN_L = 1u << 22;

constexpr size_t WS_GEN = 0;
constexpr size_t WS_CNT = 4096;
constexpr size_t WS_QG = 16384;
constexpr size_t WS_PG = WS_QG + 32 * 8 * 1024;
constexpr size_t WS_YG = WS_PG + 32 * 8 * 1152;

constexpr int L_CNT1 = 0, L_CNT2 = 4, L_QFLAG = 8, L_QCNT = 12, L_ENDF = 16, L_DONE = 20;
constexpr int L_TAB = 32;
constexpr int L_Q = NTHR * 4;
constexpr int L_ML = L_Q + 256;
constexpr int L_ML2 = L_ML + 64;
constexpr int L_REC = L_ML2 + 64;
constexpr int L_PG = L_REC + 8 * 128 * 4;
constexpr int L_YP = L_PG + 8 * 128 * 4;
constexpr int L_QP = L_YP + 8 * 128 * 4;
constexpr int L_WO = L_QP + 4 * 128 * 4;
constexpr int L_END = L_WO + 32768;
constexpr int LDS_BYTES = 84 * 1024;
static_assert(L_END <= LDS_BYTES, "lds");
static_assert(MAXU + 8 <= NTHR, "table init");
static_assert(KMAX * TPC <= 64 && (TPB - N1) % TPC == 0 && LA * TPC <= TPB - N1 && LA * TPC > (int)LOOK && N1 >= NPRE * NWS, "pool");

#define RLX_AGENT __ATOMIC_RELAXED, __HIP_MEMORY_SCOPE_AGENT

__device__ __forceinline__ float dot2(unsigned a, unsigned b, float acc) {
  return __builtin_amdgcn_fdot2_f32_bf16(__builtin_bit_cast(bf16x2, a), __builtin_bit_cast(bf16x2, b), acc, false);
}
template <int CTRL> __device__ __forceinline__ float dpp(float x) {
  return __builtin_bit_cast(float, __builtin_amdgcn_mov_dpp(__builtin_bit_cast(int, x), CTRL, 0xf, 0xf, true));
}
__device__ __forceinline__ float rowsum16(float x) {
  x += dpp<0x128>(x); x += dpp<0x141>(x); x += dpp<0x4E>(x); x += dpp<0xB1>(x); return x;
}
__device__ __forceinline__ float swap16_add(float a, float b) {
  auto s = __builtin_amdgcn_permlane16_swap(__float_as_uint(a), __float_as_uint(b), false, false);
  return __uint_as_float(s[0]) + __uint_as_float(s[1]);
}
__device__ __forceinline__ float swap32_add(float a, float b) {
  auto s = __builtin_amdgcn_permlane32_swap(__float_as_uint(a), __float_as_uint(b), false, false);
  return __uint_as_float(s[0]) + __uint_as_float(s[1]);
}
__device__ __forceinline__ float xrow_sum(float x) { x = swap16_add(x, x); return swap32_add(x, x); }
__device__ __forceinline__ float xrow_max(float x) {
  auto s = __builtin_amdgcn_permlane16_swap(__float_as_uint(x), __float_as_uint(x), false, false);
  x = fmaxf(__uint_as_float(s[0]), __uint_as_float(s[1]));
  auto t = __builtin_amdgcn_permlane32_swap(__float_as_uint(x), __float_as_uint(x), false, false);
  return fmaxf(__uint_as_float(t[0]), __uint_as_float(t[1]));
}
__device__ __forceinline__ f32x2 treduce8(const float (&t)[8]) {
  f32x2 o;
  o[0] = swap32_add(swap16_add(t[0], t[2]), swap16_add(t[4], t[6]));
  o[1] = swap32_add(swap16_add(t[1], t[3]), swap16_add(t[5], t[7]));
  return o;
}
__device__ __forceinline__ float bf_lo(unsigned u) { return __builtin_bit_cast(float, u << 16); }
__device__ __forceinline__ float bf_hi(unsigned u) { return __builtin_bit_cast(float, u & 0xffff0000u); }
__device__ __forceinline__ unsigned f2bf(float f) { __bf16 b = (__bf16)f; return (unsigned)__builtin_bit_cast(unsigned short, b); }
__device__ __forceinline__ u32x4 bld_nt(rsrc_t r, unsigned voff, unsigned soff) { return __builtin_amdgcn_raw_buffer_load_b128(r, voff, soff, 2); }
__device__ __forceinline__ void st_gran(unsigned char* p, unsigned tag, float v) {
  __hip_atomic_store((gu64*)p, ((unsigned long long)tag << 32) | __float_as_uint(v), RLX_AGENT);
}
__device__ __forceinline__ unsigned long long ld_gran(const unsigned char* p) { return __hip_atomic_load((gu64*)p, RLX_AGENT); }
__device__ __forceinline__ bool tag_ok(unsigned long long g, unsigned tag) { return (unsigned)(g >> 32) == tag; }
__device__ __forceinline__ float gval(unsigned long long g) { return __uint_as_float((unsigned)g); }

__device__ __forceinline__ unsigned lds_ticket(unsigned addr) {
  unsigned r, t0, t1;
  asm volatile("v_mov_b32 %1, %3\n\tv_mov_b32 %2, 1\n\ts_mov_b64 exec, 1\n\tds_add_rtn_u32 %1, %1, %2\n\ts_waitcnt lgkmcnt(0)\n\ts_mov_b64 exec, -1\n\tv_readfirstlane_b32 %0, %1"
               : "=s"(r), "=&v"(t0), "=&v"(t1) : "s"(addr) : "memory");
  return r;
}
__device__ __forceinline__ unsigned lds_rd(unsigned addr) {
  unsigned r, t0;
  asm volatile("v_mov_b32 %1, %2\n\tds_read_b32 %1, %1\n\ts_waitcnt lgkmcnt(0)\n\tv_readfirstlane_b32 %0, %1" : "=s"(r), "=&v"(t0) : "s"(addr) : "memory");
  return r;
}
__device__ __forceinline__ void lds_wr(unsigned addr, unsigned v) { *(lds_vu32*)(lds_u8*)(size_t)addr = v; }
#define WG_BARRIER() do { __builtin_amdgcn_sched_barrier(0); asm volatile("s_waitcnt lgkmcnt(0)\n\ts_barrier" ::: "memory"); __builtin_amdgcn_sched_barrier(0); } while (0)

__global__ void __launch_bounds__(NTHR, 3)
gqa_decode_kernel(const unsigned* __restrict__ xin, const unsigned char* __restrict__ kc, const unsigned char* __restrict__ vc,
                  const unsigned char* __restrict__ wq, const unsigned char* __restrict__ wo, unsigned* __restrict__ out, unsigned char* __restrict__ ws) {
  extern __shared__ __attribute__((aligned(16))) unsigned char smem[];
  const int tid = threadIdx.x, lane = tid & 63;
  const int wave = __builtin_amdgcn_readfirstlane(tid >> 6);
  const int b = blockIdx.x, x = b & 7, j = b >> 3;
  const int r = lane >> 4, c = lane & 15;
  const unsigned lbase = (unsigned)(size_t)(lds_u8*)smem;
#define LDS_INIT_BARRIER() do { \
    const int u_ = tid - 8; \
    reinterpret_cast<unsigned*>(smem)[tid] = u_ < 0 ? 0u : (u_ < LA * TPC ? j * HEADB + x * (1u << 20) + (N1 + (unsigned)u_) * TB : NOTREADY); \
    WG_BARRIER(); } while (0)
  unsigned tag;

  if (wave == NWS) {
    __builtin_amdgcn_s_setprio(3);
    unsigned char* cntp = ws + WS_CNT + j * 256;
    if (x == 0 && lane == 0) __hip_atomic_store((gu32*)cntp, 0u, RLX_AGENT);
    const unsigned gen = __hip_atomic_load((gu32*)(ws + WS_GEN), RLX_AGENT);
    LDS_INIT_BARRIER();
    tag = __builtin_amdgcn_readfirstlane(gen) + 1u;
    _Pragma("nounroll") for (unsigned spins = 0; spins < SPIN_L; ++spins) { if (lds_rd(lbase + L_QCNT) >= 4u) break; }
    {
      const float* qp = reinterpret_cast<const float*>(smem + L_QP);
      const float v0 = (qp[lane] + qp[128 + lane]) + (qp[256 + lane] + qp[384 + lane]);
      const float v1 = (qp[64 + lane] + qp[128 + 64 + lane]) + (qp[256 + 64 + lane] + qp[384 + 64 + lane]);
      unsigned char* qg = ws + WS_QG + ((size_t)(j * 8 + x) * 128 + lane) * 8;
      st_gran(qg, tag, v0); st_gran(qg + 512, tag, v1);
    }
    {
      const unsigned char* qg = ws + WS_QG + ((size_t)j * 8 * 128 + lane) * 8;
      float q0 = 0.f, q1 = 0.f;
      _Pragma("nounroll") for (unsigned spins = 0; spins < SPIN_G; ++spins) {
        unsigned long long g0[8], g1[8];
#pragma unroll
        for (int p = 0; p < 8; ++p) { g0[p] = ld_gran(qg + p * 1024); g1[p] = ld_gran(qg + p * 1024 + 512); }
        bool ok = true; q0 = 0.f; q1 = 0.f;
#pragma unroll
        for (int p = 0; p < 8; ++p) { ok &= tag_ok(g0[p], tag) & tag_ok(g1[p], tag); q0 += gval(g0[p]); q1 += gval(g1[p]); }
        if (__all(ok)) break;
      }
      reinterpret_cast<unsigned short*>(smem + L_Q)[lane] = (unsigned short)f2bf(q0);
      reinterpret_cast<unsigned short*>(smem + L_Q)[lane + 64] = (unsigned short)f2bf(q1);
      asm volatile("s_waitcnt lgkmcnt(0)" ::: "memory");
      if (lane == 0) lds_wr(lbase + L_QFLAG, 1u);
    }
    __builtin_amdgcn_s_setprio(0);
    if (x == 0) {
      asm volatile("s_waitcnt vmcnt(0)" ::: "memory");
      if (lane == 0) __hip_atomic_store((gu32*)(cntp + 128), tag, RLX_AGENT);
    }
    _Pragma("nounroll") for (unsigned spins = 0; spins < SPIN_G; ++spins) if (__hip_atomic_load((gu32*)(cntp + 128), RLX_AGENT) == tag) break;
    {
      rsrc_t cr = __builtin_amdgcn_make_buffer_rsrc((void*)cntp, 0, 4, 0x00020000);
      unsigned filled = LA * TPC;
      _Pragma("nounroll") for (;;) {
        unsigned taken = 0;
        _Pragma("nounroll") for (unsigned spins = 0; spins < SPIN_L; ++spins) { taken = lds_rd(lbase + L_CNT2); if (taken + LOOK >= filled) break; __builtin_amdgcn_s_sleep(2); }
        unsigned k = taken + LOOK >= filled ? (taken + LOOK - filled) / TPC + 1u : 1u; k = k > KMAX ? KMAX : k;
        const unsigned room = ((unsigned)TABN - filled) / TPC; k = k > room ? room : k;
        const unsigned gv = (unsigned)__builtin_amdgcn_raw_ptr_buffer_atomic_add_i32(1, cr, (unsigned)lane < k ? 0u : 64u, 0, 0) + LA * 8;
        const unsigned g = (unsigned)__builtin_amdgcn_ds_bpermute((lane / TPC) * 4, (int)gv);
        const bool act = (unsigned)lane < k * TPC;
        const bool good = g < (unsigned)PC;
        if (act) lds_wr(lbase + L_TAB + 4 * (filled + lane), good ? j * HEADB + (g & 7) * (1u << 20) + (N1 + (g >> 3) * TPC + (lane % TPC)) * TB : ENDV);
        filled += k * TPC;
        if (k == 0 || __any(act && !good)) {
          if (lane == 0) lds_wr(lbase + L_ENDF, 1u);
          _Pragma("nounroll") for (unsigned u = filled + lane; u < (unsigned)MAXU; u += 64) lds_wr(lbase + L_TAB + 4 * u, ENDV);
          break;
        }
      }
    }
    _Pragma("nounroll") for (unsigned spins = 0; spins < SPIN_L; ++spins) { if (lds_rd(lbase + L_DONE) >= (unsigned)NWS) break; }
    {
      const f32x4 m0 = *reinterpret_cast<const f32x4*>(smem + L_ML), m1 = *reinterpret_cast<const f32x4*>(smem + L_ML + 16);
      const f32x4 l0 = *reinterpret_cast<const f32x4*>(smem + L_ML + 32), l1 = *reinterpret_cast<const f32x4*>(smem + L_ML + 48);
      const float* rec = reinterpret_cast<const float*>(smem + L_REC) + lane;
      float oa[8], ob[8];
#pragma unroll
      for (int w = 0; w < 8; ++w) { oa[w] = rec[w * 128]; ob[w] = rec[w * 128 + 64]; }
      __builtin_amdgcn_sched_barrier(0);
      const float M = fmaxf(fmaxf(fmaxf(m0[0], m0[1]), fmaxf(m0[2], m0[3])), fmaxf(fmaxf(m1[0], m1[1]), fmaxf(m1[2], m1[3])));
      float L = 0.f, Oa = 0.f, Ob = 0.f;
#pragma unroll
      for (int w = 0; w < 8; ++w) {
        const float e = __builtin_amdgcn_exp2f((w < 4 ? m0[w & 3] : m1[w & 3]) - M);
        L = fmaf(e, w < 4 ? l0[w & 3] : l1[w & 3], L); Oa = fmaf(e, oa[w], Oa); Ob = fmaf(e, ob[w], Ob);
      }
      unsigned char* pg = ws + WS_PG + (size_t)(j * 8 + x) * 1152 + lane * 8;
      st_gran(pg, tag, Oa); st_gran(pg + 512, tag, Ob);
      if (lane < 16) st_gran(pg + 1024, tag, lane == 0 ? M : L);
    }
  } else {
    rsrc_t kr = __builtin_amdgcn_make_buffer_rsrc((void*)kc, 0, KVB, 0x00020000);
    rsrc_t vr = __builtin_amdgcn_make_buffer_rsrc((void*)vc, 0, KVB, 0x00020000);
    const unsigned loff = lane * 16;
    const unsigned base1 = j * HEADB + x * (1u << 20);
    u32x4 kb[NPRE][4], vb[NPRE][4];
#define ISSUE_K(S, OFF) do { _Pragma("unroll") for (int i = 0; i < 4; ++i) kb[S][i] = bld_nt(kr, loff + i * 1024, (OFF)); __builtin_amdgcn_sched_barrier(0); } while (0)
#define ISSUE_V(S, OFF) do { _Pragma("unroll") for (int i = 0; i < 4; ++i) vb[S][i] = bld_nt(vr, loff + i * 1024, (OFF)); __builtin_amdgcn_sched_barrier(0); } while (0)
#define ISSUE(S, OFF) do { const unsigned o_ = (OFF); ISSUE_K(S, o_); ISSUE_V(S, o_); } while (0)
    if (wave >= NWD) {
      __builtin_amdgcn_s_setprio(3);
      const int row0 = 80 * x + 20 * (wave - NWD);
      rsrc_t wqr = __builtin_amdgcn_make_buffer_rsrc((void*)wq, 0, D_MODEL * NH * DH * 2, 0x00020000);
      const unsigned wq_voff = (unsigned)r * (NH * DH * 2) + (unsigned)c * 16;
      const unsigned wq_soff = (unsigned)row0 * (NH * DH * 2) + (unsigned)j * (DH * 2);
      unsigned xv[5]; u32x4 w[5];
#pragma unroll
      for (int i = 0; i < 5; ++i) xv[i] = xin[(row0 + 4 * i + r) >> 1];
#pragma unroll
      for (int i = 0; i < 5; ++i) w[i] = bld_nt(wqr, wq_voff, wq_soff + (unsigned)i * 4 * (NH * DH * 2));
      __builtin_amdgcn_sched_barrier(0);
      LDS_INIT_BARRIER();
      float qa[8];
#pragma unroll
      for (int k = 0; k < 8; ++k) qa[k] = 0.f;
#pragma unroll
      for (int i = 0; i < 5; ++i) {
        const unsigned xl = (r & 1) ? xv[i] >> 16 : xv[i] & 0xffffu, xh = xl << 16;
#pragma unroll
        for (int k = 0; k < 4; ++k) { qa[2 * k] = dot2(w[i][k], xl, qa[2 * k]); qa[2 * k + 1] = dot2(w[i][k], xh, qa[2 * k + 1]); }
      }
      *reinterpret_cast<f32x2*>(smem + L_QP + ((wave - NWD) * 128 + 8 * c + 2 * r) * 4) = treduce8(qa);
      asm volatile("s_waitcnt lgkmcnt(0)" ::: "memory");
      (void)lds_ticket(lbase + L_QCNT);
      __builtin_amdgcn_s_setprio(1);
      __builtin_amdgcn_sched_barrier(0);
      ISSUE_K(0, base1 + wave * TB);
    } else {
      ISSUE_K(0, base1 + wave * TB);
      LDS_INIT_BARRIER();
    }
    ISSUE_V(0, base1 + wave * TB);
#pragma unroll
    for (int s = 1; s < NPRE; ++s) ISSUE(s, base1 + (s * NWS + wave) * TB);
    const unsigned gen = __hip_atomic_load((gu32*)(ws + WS_GEN), RLX_AGENT);
    __builtin_amdgcn_sched_barrier(0);
    _Pragma("nounroll") for (unsigned spins = 0; spins < SPIN_L; ++spins) { if (lds_rd(lbase + L_QFLAG) != 0u) break; __builtin_amdgcn_s_sleep(1); }
    const u32x4 qv = *reinterpret_cast<const u32x4*>(smem + L_Q + c * 16);
    constexpr float C2 = 0.08838834764831845f * 1.4426950408889634f;
    float m = -INFINITY, l = 0.f, acc[8];
#pragma unroll
    for (int k = 0; k < 8; ++k) acc[k] = 0.f;
    bool dyn = false;
    auto next = [&]() -> unsigned {
      if (wave >= NWD) return ENDV;
      if (!dyn) {
        const unsigned t = lds_ticket(lbase + L_CNT1);
        if (t < (unsigned)(N1 - NPRE * NWS)) return base1 + (NPRE * NWS + t) * TB;
        dyn = true;
      }
      const unsigned u = lds_ticket(lbase + L_CNT2);
      if (u >= (unsigned)MAXU) return ENDV;
      unsigned v = ENDV;
      _Pragma("nounroll") for (unsigned spins = 0; spins < SPIN_L; ++spins) { v = lds_rd(lbase + L_TAB + 4 * u); if (v != NOTREADY) break; __builtin_amdgcn_s_sleep(1); }
      return v == NOTREADY ? ENDV : v;
    };
#define CONSUME(S) do { \
      float s_[4]; \
      _Pragma("unroll") for (int i = 0; i < 4; ++i) { float d_ = dot2(kb[S][i][0], qv[0], 0.f); d_ = dot2(kb[S][i][1], qv[1], d_); d_ = dot2(kb[S][i][2], qv[2], d_); d_ = dot2(kb[S][i][3], qv[3], d_); s_[i] = rowsum16(d_); } \
      const float mn_ = fmaxf(m, fmaxf(fmaxf(s_[0], s_[1]), fmaxf(s_[2], s_[3])) * C2); \
      const float al_ = __builtin_amdgcn_exp2f(m - mn_); m = mn_; l *= al_; \
      _Pragma("unroll") for (int k = 0; k < 8; ++k) acc[k] *= al_; \
      _Pragma("unroll") for (int i = 0; i < 4; ++i) { const float p_ = __builtin_amdgcn_exp2f(fmaf(s_[i], C2, -mn_)); l += p_; \
        _Pragma("unroll") for (int k = 0; k < 4; ++k) { acc[2 * k] = fmaf(p_, bf_lo(vb[S][i][k]), acc[2 * k]); acc[2 * k + 1] = fmaf(p_, bf_hi(vb[S][i][k]), acc[2 * k + 1]); } } \
      __builtin_amdgcn_sched_barrier(0); \
    } while (0)
    bool v0, v1;
    { const unsigned off = next(); CONSUME(0); v0 = off != ENDV; ISSUE(0, off); }
    { const unsigned off = next(); CONSUME(1); v1 = off != ENDV; ISSUE(1, off); }
#pragma unroll
    for (int s = 2; s < NPRE; ++s) CONSUME(s);
    _Pragma("nounroll") for (;;) {
      if (!v0) break;
      { const unsigned off = next(); CONSUME(0); v0 = off != ENDV; ISSUE(0, off); }
      if (!v1) break;
      { const unsigned off = next(); CONSUME(1); v1 = off != ENDV; ISSUE(1, off); }
    }
    tag = __builtin_amdgcn_readfirstlane(gen) + 1u;
    const float mw = xrow_max(m);
    const float sc = __builtin_amdgcn_exp2f(m - mw);
    const float lw = xrow_sum(l * sc);
#pragma unroll
    for (int k = 0; k < 8; ++k) acc[k] *= sc;
    *reinterpret_cast<f32x2*>(smem + L_REC + (wave * 128 + 8 * c + 2 * r) * 4) = treduce8(acc);
    if (lane < 2) reinterpret_cast<float*>(smem + L_ML)[lane * 8 + wave] = lane == 0 ? mw : lw;
    asm volatile("s_waitcnt lgkmcnt(0)" ::: "memory");
    (void)lds_ticket(lbase + L_DONE);
    if (wave >= NWD && x < 5) {
      _Pragma("nounroll") for (unsigned spins = 0; spins < SPIN_L; ++spins) { if (lds_rd(lbase + L_ENDF) != 0u) break; __builtin_amdgcn_s_sleep(8); }
      rsrc_t wor = __builtin_amdgcn_make_buffer_rsrc((void*)wo, 0, NH * DH * D_MODEL * 2, 0x00020000);
      const unsigned voff = (unsigned)r * (D_MODEL * 2) + (unsigned)c * 16;
      const unsigned soff = (unsigned)(128 * j + 32 * (wave - NWD)) * (D_MODEL * 2) + (unsigned)x * 256;
      u32x4 t[8];
#pragma unroll
      for (int i = 0; i < 8; ++i) t[i] = bld_nt(wor, voff, soff + (unsigned)i * 4 * (D_MODEL * 2));
      __builtin_amdgcn_sched_barrier(0);
#pragma unroll
      for (int i = 0; i < 8; ++i) *reinterpret_cast<u32x4*>(smem + L_WO + ((wave - NWD) * 8 + i) * 1024 + lane * 16) = t[i];
    }
  }
  if (x >= 5) return;
  if (wave < NWS) {
    const unsigned char* pg = ws + WS_PG + (size_t)(j * 8 + wave) * 1152 + lane * 8;
    const unsigned char* pml = ws + WS_PG + (size_t)(j * 8 + wave) * 1152 + 1024 + (lane & 1) * 8;
    unsigned long long g0 = 0, g1 = 0, gm = 0;
    _Pragma("nounroll") for (unsigned spins = 0; spins < SPIN_G * 8u; ++spins) {
      g0 = ld_gran(pg); g1 = ld_gran(pg + 512); gm = ld_gran(pml);
      if (__all(tag_ok(g0, tag) & tag_ok(g1, tag) & tag_ok(gm, tag))) break;
    }
    float* pgl = reinterpret_cast<float*>(smem + L_PG) + wave * 128;
    pgl[lane] = gval(g0); pgl[64 + lane] = gval(g1);
    if (lane < 2) reinterpret_cast<float*>(smem + L_ML2)[lane * 8 + wave] = gval(gm);
  }
  WG_BARRIER();
  if (wave < NWS) {
    u32x4 wv[4];
#pragma unroll
    for (int i = 0; i < 4; ++i) wv[i] = *reinterpret_cast<const u32x4*>(smem + L_WO + (wave * 4 + i) * 1024 + lane * 16);
    const f32x2 mm = *reinterpret_cast<const f32x2*>(smem + L_ML2 + r * 8), ll = *reinterpret_cast<const f32x2*>(smem + L_ML2 + 32 + r * 8);
    const float* pgl = reinterpret_cast<const float*>(smem + L_PG) + (2 * r) * 128 + wave * 16 + c;
    const float oa = pgl[0], ob = pgl[128];
    const float M = xrow_max(fmaxf(mm[0], mm[1]));
    const float ea = __builtin_amdgcn_exp2f(mm[0] - M), eb = __builtin_amdgcn_exp2f(mm[1] - M);
    const float num = xrow_sum(fmaf(ea, oa, eb * ob)), den = xrow_sum(fmaf(ea, ll[0], eb * ll[1]));
    const unsigned obf = f2bf(num * __builtin_amdgcn_rcpf(den));
    float a[8];
#pragma unroll
    for (int k = 0; k < 8; ++k) a[k] = 0.f;
#pragma unroll
    for (int i = 0; i < 4; ++i) {
      const unsigned ol = (unsigned)__builtin_amdgcn_ds_bpermute((4 * i + r) * 4, (int)obf), oh = ol << 16;
#pragma unroll
      for (int k = 0; k < 4; ++k) { a[2 * k] = dot2(wv[i][k], ol, a[2 * k]); a[2 * k + 1] = dot2(wv[i][k], oh, a[2 * k + 1]); }
    }
    *reinterpret_cast<f32x2*>(smem + L_YP + (wave * 128 + 8 * c + 2 * r) * 4) = treduce8(a);
  }
  WG_BARRIER();
  if (tid < 128) {
    const float* yp = reinterpret_cast<const float*>(smem + L_YP);
    float y = 0.f;
#pragma unroll
    for (int w = 0; w < 8; ++w) y += yp[w * 128 + tid];
    st_gran(ws + WS_YG + (((size_t)x * 32 + j) * 128 + tid) * 8, tag, y);
  }
  if (wave == 2) {
    const unsigned char* yg = ws + WS_YG + (((size_t)x * 32 + (lane >> 1)) * 128 + 4 * j + (lane & 1) * 2) * 8;
    unsigned long long g0 = 0, g1 = 0;
    _Pragma("nounroll") for (unsigned spins = 0; spins < SPIN_G * 8u; ++spins) {
      g0 = ld_gran(yg); g1 = ld_gran(yg + 8);
      if (__all(tag_ok(g0, tag) & tag_ok(g1, tag))) break;
    }
    float y0 = gval(g0), y1 = gval(g1);
    y0 += dpp<0x4E>(y0); y1 += dpp<0x4E>(y1);
    y0 += dpp<0x124>(y0); y1 += dpp<0x124>(y1);
    y0 += dpp<0x128>(y0); y1 += dpp<0x128>(y1);
    y0 = xrow_sum(y0); y1 = xrow_sum(y1);
    if (lane < 2) out[64 * x + 2 * j + lane] = f2bf(y0) | (f2bf(y1) << 16);
    if (b == 0 && lane == 0) __hip_atomic_store((gu32*)(ws + WS_GEN), tag, RLX_AGENT);
  }
}

extern "C" void kernel_launch(void* const* d_in, const int* in_sizes, int n_in, void* d_out, int out_size, void* d_ws, size_t ws_size, hipStream_t stream) {
  (void)in_sizes; (void)n_in; (void)out_size; (void)ws_size;
  (void)hipFuncSetAttribute((const void*)gqa_decode_kernel, hipFuncAttributeMaxDynamicSharedMemorySize, LDS_BYTES);
  gqa_decode_kernel<<<256, NTHR, LDS_BYTES, stream>>>((const unsigned*)d_in[0], (const unsigned char*)d_in[1], (const unsigned char*)d_in[2],
                                                     (const unsigned char*)d_in[3], (const unsigned char*)d_in[4], (unsigned*)d_out, (unsigned char*)d_ws);
}
```

```cpp
#include <hip/hip_runtime.h>
#include <hip/hip_bf16.h>
#include <stdint.h>

typedef __attribute__((ext_vector_type(4))) unsigned u32x4;
typedef __attribute__((ext_vector_type(4))) float f32x4;
typedef __attribute__((ext_vector_type(2))) float f32x2;
typedef __attribute__((ext_vector_type(2))) __bf16 bf16x2;
typedef __amdgpu_buffer_rsrc_t rsrc_t;
typedef __attribute__((address_space(1))) unsigned long long gu64;
typedef __attribute__((address_space(1))) unsigned gu32;
typedef __attribute__((address_space(3))) volatile unsigned lds_vu32;
typedef __attribute__((address_space(3))) unsigned char lds_u8;

constexpr int D_MODEL = 640, NH = 32, DH = 128;
constexpr int NWS = 8;
constexpr int NWD = 4;
constexpr int NTHR = (NWS + 1) * 64;
constexpr unsigned TB = 4096;
constexpr int TPB = 256;
constexpr int N1 = 176;
constexpr int TPC = 2;
constexpr int LA = 6;
constexpr unsigned LOOK = 10;
constexpr unsigned KMAX = 4;
constexpr int NPRE = 3;
constexpr int CPB = (TPB - N1) / TPC;
constexpr int PC = 8 * CPB;
constexpr int TABN = PC * TPC < 528 ? PC * TPC : 528;
constexpr int MAXU = TABN + 32;
constexpr unsigned HEADB = 8u << 20, KVB = 256u << 20;
constexpr unsigned NOTREADY = 0xffffffffu, ENDV = 0x80000000u;
constexpr unsigned SPIN_G = 1u << 17;
constexpr unsigned SPIN_L = 1u << 22;

constexpr size_t WS_GEN = 0;
constexpr size_t WS_CNT = 4096;
constexpr size_t WS_QG = 16384;
constexpr size_t WS_PG = WS_QG + 32 * 8 * 1024;
constexpr size_t WS_YG = WS_PG + 32 * 8 * 1152;

constexpr int L_CNT1 = 0, L_CNT2 = 4, L_QFLAG = 8, L_QCNT = 12, L_ENDF = 16;
constexpr int L_TAB = 32;
constexpr int L_Q = NTHR * 4;
constexpr int L_ML = L_Q + 256;
constexpr int L_ML2 = L_ML + 64;
constexpr int L_REC = L_ML2 + 64;
constexpr int L_PG = L_REC + 8 * 128 * 4;
constexpr int L_YP = L_PG + 8 * 128 * 4;
constexpr int L_QP = L_YP + 8 * 128 * 4;
constexpr int L_WO = L_QP + 4 * 128 * 4;
constexpr int L_END = L_WO + 32768;
constexpr int LDS_BYTES = 84 * 1024;
static_assert(L_END <= LDS_BYTES, "lds");
static_assert(MAXU + 8 <= NTHR, "table init");
static_assert(KMAX * TPC <= 64 && (TPB - N1) % TPC == 0 && LA * TPC <= TPB - N1 && LA * TPC > (int)LOOK && N1 >= NPRE * NWS, "pool");

#define RLX_AGENT __ATOMIC_RELAXED, __HIP_MEMORY_SCOPE_AGENT

__device__ __forceinline__ float dot2(unsigned a, unsigned b, float acc) {
  return __builtin_amdgcn_fdot2_f32_bf16(__builtin_bit_cast(bf16x2, a), __builtin_bit_cast(bf16x2, b), acc, false);
}
template <int CTRL> __device__ __forceinline__ float dpp(float x) {
  return __builtin_bit_cast(float, __builtin_amdgcn_mov_dpp(__builtin_bit_cast(int, x), CTRL, 0xf, 0xf, true));
}
__device__ __forceinline__ float rowsum16(float x) {
  x += dpp<0x128>(x); x += dpp<0x141>(x); x += dpp<0x4E>(x); x += dpp<0xB1>(x); return x;
}
__device__ __forceinline__ float swap16_add(float a, float b) {
  auto s = __builtin_amdgcn_permlane16_swap(__float_as_uint(a), __float_as_uint(b), false, false);
  return __uint_as_float(s[0]) + __uint_as_float(s[1]);
}
__device__ __forceinline__ float swap32_add(float a, float b) {
  auto s = __builtin_amdgcn_permlane32_swap(__float_as_uint(a), __float_as_uint(b), false, false);
  return __uint_as_float(s[0]) + __uint_as_float(s[1]);
}
__device__ __forceinline__ float xrow_sum(float x) { x = swap16_add(x, x); return swap32_add(x, x); }
__device__ __forceinline__ float xrow_max(float x) {
  auto s = __builtin_amdgcn_permlane16_swap(__float_as_uint(x), __float_as_uint(x), false, false);
  x = fmaxf(__uint_as_float(s[0]), __uint_as_float(s[1]));
  auto t = __builtin_amdgcn_permlane32_swap(__float_as_uint(x), __float_as_uint(x), false, false);
  return fmaxf(__uint_as_float(t[0]), __uint_as_float(t[1]));
}
__device__ __forceinline__ f32x2 treduce8(const float (&t)[8]) {
  f32x2 o;
  o[0] = swap32_add(swap16_add(t[0], t[2]), swap16_add(t[4], t[6]));
  o[1] = swap32_add(swap16_add(t[1], t[3]), swap16_add(t[5], t[7]));
  return o;
}
__device__ __forceinline__ float bf_lo(unsigned u) { return __builtin_bit_cast(float, u << 16); }
__device__ __forceinline__ float bf_hi(unsigned u) { return __builtin_bit_cast(float, u & 0xffff0000u); }
__device__ __forceinline__ unsigned f2bf(float f) { __bf16 b = (__bf16)f; return (unsigned)__builtin_bit_cast(unsigned short, b); }
__device__ __forceinline__ u32x4 bld_nt(rsrc_t r, unsigned voff, unsigned soff) { return __builtin_amdgcn_raw_buffer_load_b128(r, voff, soff, 2); }
__device__ __forceinline__ void st_gran(unsigned char* p, unsigned tag, float v) {
  __hip_atomic_store((gu64*)p, ((unsigned long long)tag << 32) | __float_as_uint(v), RLX_AGENT);
}
__device__ __forceinline__ unsigned long long ld_gran(const unsigned char* p) { return __hip_atomic_load((gu64*)p, RLX_AGENT); }
__device__ __forceinline__ bool tag_ok(unsigned long long g, unsigned tag) { return (unsigned)(g >> 32) == tag; }
__device__ __forceinline__ float gval(unsigned long long g) { return __uint_as_float((unsigned)g); }

#define POLL_CHECK(P) \
    "v_lshrrev_b64 %[t], 32, %[" #P "0]\n\tv_cmp_eq_u64 vcc, %[tg], %[t]\n\t" \
    "v_lshrrev_b64 %[t], 32, %[" #P "1]\n\tv_cmp_eq_u64 %[m], %[tg], %[t]\n\ts_and_b64 vcc, vcc, %[m]\n\t"
#define POLL_CHECK_X(P) "v_lshrrev_b64 %[t], 32, %[" #P "2]\n\tv_cmp_eq_u64 %[m], %[tg], %[t]\n\ts_and_b64 vcc, vcc, %[m]\n\t"
__device__ __forceinline__ unsigned poll3(unsigned long long& a0, unsigned long long& a1, unsigned long long& a2,
                                          unsigned long long& b0, unsigned long long& b1, unsigned long long& b2,
                                          const unsigned char* p, const unsigned char* p2, unsigned tag) {
  unsigned which, cnt; unsigned long long t, m; const unsigned long long tg = tag;
  asm volatile(
    "s_waitcnt vmcnt(0)\n\t"
    "global_load_dwordx2 %[a0], %[p], off sc1\n\tglobal_load_dwordx2 %[a1], %[p], off offset:512 sc1\n\tglobal_load_dwordx2 %[a2], %[p2], off sc1\n\t"
    "s_sleep 8\n\t"
    "global_load_dwordx2 %[b0], %[p], off sc1\n\tglobal_load_dwordx2 %[b1], %[p], off offset:512 sc1\n\tglobal_load_dwordx2 %[b2], %[p2], off sc1\n\t"
    "s_mov_b32 %[cnt], 0x100000\n"
    "1:\n\t"
    "s_mov_b32 %[w], 0\n\ts_waitcnt vmcnt(3)\n\t"
    POLL_CHECK(a) POLL_CHECK_X(a)
    "s_cmp_eq_u64 vcc, exec\n\ts_cbranch_scc1 2f\n\t"
    "global_load_dwordx2 %[a0], %[p], off sc1\n\tglobal_load_dwordx2 %[a1], %[p], off offset:512 sc1\n\tglobal_load_dwordx2 %[a2], %[p2], off sc1\n\t"
    "s_mov_b32 %[w], 1\n\ts_waitcnt vmcnt(3)\n\t"
    POLL_CHECK(b) POLL_CHECK_X(b)
    "s_cmp_eq_u64 vcc, exec\n\ts_cbranch_scc1 2f\n\t"
    "global_load_dwordx2 %[b0], %[p], off sc1\n\tglobal_load_dwordx2 %[b1], %[p], off offset:512 sc1\n\tglobal_load_dwordx2 %[b2], %[p2], off sc1\n\t"
    "s_sub_u32 %[cnt], %[cnt], 1\n\ts_cmp_lg_u32 %[cnt], 0\n\ts_cbranch_scc1 1b\n"
    "2:"
    : [a0] "=&v"(a0), [a1] "=&v"(a1), [a2] "=&v"(a2), [b0] "=&v"(b0), [b1] "=&v"(b1), [b2] "=&v"(b2), [w] "=&s"(which), [cnt] "=&s"(cnt), [t] "=&v"(t), [m] "=&s"(m)
    : [p] "v"(p), [p2] "v"(p2), [tg] "s"(tg) : "vcc", "scc", "memory");
  return which;
}
__device__ __forceinline__ unsigned poll2(unsigned long long& a0, unsigned long long& a1, unsigned long long& b0, unsigned long long& b1,
                                          const unsigned char* p, unsigned tag) {
  unsigned which, cnt; unsigned long long t, m; const unsigned long long tg = tag;
  asm volatile(
    "s_waitcnt vmcnt(0)\n\t"
    "global_load_dwordx2 %[a0], %[p], off sc1\n\tglobal_load_dwordx2 %[a1], %[p], off offset:8 sc1\n\t"
    "s_sleep 8\n\t"
    "global_load_dwordx2 %[b0], %[p], off sc1\n\tglobal_load_dwordx2 %[b1], %[p], off offset:8 sc1\n\t"
    "s_mov_b32 %[cnt], 0x100000\n"
    "1:\n\t"
    "s_mov_b32 %[w], 0\n\ts_waitcnt vmcnt(2)\n\t"
    POLL_CHECK(a)
    "s_cmp_eq_u64 vcc, exec\n\ts_cbranch_scc1 2f\n\t"
    "global_load_dwordx2 %[a0], %[p], off sc1\n\tglobal_load_dwordx2 %[a1], %[p], off offset:8 sc1\n\t"
    "s_mov_b32 %[w], 1\n\ts_waitcnt vmcnt(2)\n\t"
    POLL_CHECK(b)
    "s_cmp_eq_u64 vcc, exec\n\ts_cbranch_scc1 2f\n\t"
    "global_load_dwordx2 %[b0], %[p], off sc1\n\tglobal_load_dwordx2 %[b1], %[p], off offset:8 sc1\n\t"
    "s_sub_u32 %[cnt], %[cnt], 1\n\ts_cmp_lg_u32 %[cnt], 0\n\ts_cbranch_scc1 1b\n"
    "2:"
    : [a0] "=&v"(a0), [a1] "=&v"(a1), [b0] "=&v"(b0), [b1] "=&v"(b1), [w] "=&s"(which), [cnt] "=&s"(cnt), [t] "=&v"(t), [m] "=&s"(m)
    : [p] "v"(p), [tg] "s"(tg) : "vcc", "scc", "memory");
  return which;
}
#define POLL_DRAIN3(a0, a1, a2, b0, b1, b2) asm volatile("s_waitcnt vmcnt(0)" : "+v"(a0), "+v"(a1), "+v"(a2), "+v"(b0), "+v"(b1), "+v"(b2) :: "memory")
#define POLL_DRAIN2(a0, a1, b0, b1) asm volatile("s_waitcnt vmcnt(0)" : "+v"(a0), "+v"(a1), "+v"(b0), "+v"(b1) :: "memory")
__device__ __forceinline__ unsigned lds_ticket(unsigned addr) {
  unsigned r, t0, t1;
  asm volatile("v_mov_b32 %1, %3\n\tv_mov_b32 %2, 1\n\ts_mov_b64 exec, 1\n\tds_add_rtn_u32 %1, %1, %2\n\ts_waitcnt lgkmcnt(0)\n\ts_mov_b64 exec, -1\n\tv_readfirstlane_b32 %0, %1"
               : "=s"(r), "=&v"(t0), "=&v"(t1) : "s"(addr) : "memory");
  return r;
}
__device__ __forceinline__ unsigned lds_rd(unsigned addr) {
  unsigned r, t0;
  asm volatile("v_mov_b32 %1, %2\n\tds_read_b32 %1, %1\n\ts_waitcnt lgkmcnt(0)\n\tv_readfirstlane_b32 %0, %1" : "=s"(r), "=&v"(t0) : "s"(addr) : "memory");
  return r;
}
__device__ __forceinline__ void lds_wr(unsigned addr, unsigned v) { *(lds_vu32*)(lds_u8*)(size_t)addr = v; }
#define WG_BARRIER() do { __builtin_amdgcn_sched_barrier(0); asm volatile("s_waitcnt lgkmcnt(0)\n\ts_barrier" ::: "memory"); __builtin_amdgcn_sched_barrier(0); } while (0)

__global__ void __launch_bounds__(NTHR, 3)
gqa_decode_kernel(const unsigned* __restrict__ xin, const unsigned char* __restrict__ kc, const unsigned char* __restrict__ vc,
                  const unsigned char* __restrict__ wq, const unsigned char* __restrict__ wo, unsigned* __restrict__ out, unsigned char* __restrict__ ws) {
  extern __shared__ __attribute__((aligned(16))) unsigned char smem[];
  const int tid = threadIdx.x, lane = tid & 63;
  const int wave = __builtin_amdgcn_readfirstlane(tid >> 6);
  const int b = blockIdx.x, x = b & 7, j = b >> 3;
  const int r = lane >> 4, c = lane & 15;
  const unsigned lbase = (unsigned)(size_t)(lds_u8*)smem;
#define LDS_INIT_BARRIER() do { \
    const int u_ = tid - 8; \
    reinterpret_cast<unsigned*>(smem)[tid] = u_ < 0 ? 0u : (u_ < LA * TPC ? j * HEADB + x * (1u << 20) + (N1 + (unsigned)u_) * TB : NOTREADY); \
    WG_BARRIER(); } while (0)
  unsigned tag;

  if (wave == NWS) {
    __builtin_amdgcn_s_setprio(3);
    unsigned char* cntp = ws + WS_CNT + j * 256;
    if (x == 0 && lane == 0) __hip_atomic_store((gu32*)cntp, 0u, RLX_AGENT);
    const unsigned gen = __hip_atomic_load((gu32*)(ws + WS_GEN), RLX_AGENT);
    LDS_INIT_BARRIER();
    tag = __builtin_amdgcn_readfirstlane(gen) + 1u;
    _Pragma("nounroll") for (unsigned spins = 0; spins < SPIN_L; ++spins) { if (lds_rd(lbase + L_QCNT) >= 4u) break; }
    {
      const float* qp = reinterpret_cast<const float*>(smem + L_QP);
      const float v0 = (qp[lane] + qp[128 + lane]) + (qp[256 + lane] + qp[384 + lane]);
      const float v1 = (qp[64 + lane] + qp[128 + 64 + lane]) + (qp[256 + 64 + lane] + qp[384 + 64 + lane]);
      unsigned char* qg = ws + WS_QG + ((size_t)(j * 8 + x) * 128 + lane) * 8;
      st_gran(qg, tag, v0); st_gran(qg + 512, tag, v1);
    }
    {
      const unsigned char* qg = ws + WS_QG + ((size_t)j * 8 * 128 + lane) * 8;
      float q0 = 0.f, q1 = 0.f;
      _Pragma("nounroll") for (unsigned spins = 0; spins < SPIN_G; ++spins) {
        unsigned long long g0[8], g1[8];
#pragma unroll
        for (int p = 0; p < 8; ++p) { g0[p] = ld_gran(qg + p * 1024); g1[p] = ld_gran(qg + p * 1024 + 512); }
        bool ok = true; q0 = 0.f; q1 = 0.f;
#pragma unroll
        for (int p = 0; p < 8; ++p) { ok &= tag_ok(g0[p], tag) & tag_ok(g1[p], tag); q0 += gval(g0[p]); q1 += gval(g1[p]); }
        if (__all(ok)) break;
      }
      reinterpret_cast<unsigned short*>(smem + L_Q)[lane] = (unsigned short)f2bf(q0);
      reinterpret_cast<unsigned short*>(smem + L_Q)[lane + 64] = (unsigned short)f2bf(q1);
      asm volatile("s_waitcnt lgkmcnt(0)" ::: "memory");
      if (lane == 0) lds_wr(lbase + L_QFLAG, 1u);
    }
    __builtin_amdgcn_s_setprio(0);
    if (x == 0) {
      asm volatile("s_waitcnt vmcnt(0)" ::: "memory");
      if (lane == 0) __hip_atomic_store((gu32*)(cntp + 128), tag, RLX_AGENT);
    }
    _Pragma("nounroll") for (unsigned spins = 0; spins < SPIN_G; ++spins) if (__hip_atomic_load((gu32*)(cntp + 128), RLX_AGENT) == tag) break;
    {
      rsrc_t cr = __builtin_amdgcn_make_buffer_rsrc((void*)cntp, 0, 4, 0x00020000);
      unsigned filled = LA * TPC;
      _Pragma("nounroll") for (;;) {
        unsigned taken = 0;
        _Pragma("nounroll") for (unsigned spins = 0; spins < SPIN_L; ++spins) { taken = lds_rd(lbase + L_CNT2); if (taken + LOOK >= filled) break; __builtin_amdgcn_s_sleep(2); }
        unsigned k = taken + LOOK >= filled ? (taken + LOOK - filled) / TPC + 1u : 1u; k = k > KMAX ? KMAX : k;
        const unsigned room = ((unsigned)TABN - filled) / TPC; k = k > room ? room : k;
        const unsigned gv = (unsigned)__builtin_amdgcn_raw_ptr_buffer_atomic_add_i32(1, cr, (unsigned)lane < k ? 0u : 64u, 0, 0) + LA * 8;
        const unsigned g = (unsigned)__builtin_amdgcn_ds_bpermute((lane / TPC) * 4, (int)gv);
        const bool act = (unsigned)lane < k * TPC;
        const bool good = g < (unsigned)PC;
        if (act) lds_wr(lbase + L_TAB + 4 * (filled + lane), good ? j * HEADB + (g & 7) * (1u << 20) + (N1 + (g >> 3) * TPC + (lane % TPC)) * TB : ENDV);
        filled += k * TPC;
        if (k == 0 || __any(act && !good)) {
          if (lane == 0) lds_wr(lbase + L_ENDF, 1u);
          _Pragma("nounroll") for (unsigned u = filled + lane; u < (unsigned)MAXU; u += 64) lds_wr(lbase + L_TAB + 4 * u, ENDV);
          break;
        }
      }
    }
  } else {
    rsrc_t kr = __builtin_amdgcn_make_buffer_rsrc((void*)kc, 0, KVB, 0x00020000);
    rsrc_t vr = __builtin_amdgcn_make_buffer_rsrc((void*)vc, 0, KVB, 0x00020000);
    const unsigned loff = lane * 16;
    const unsigned base1 = j * HEADB + x * (1u << 20);
    u32x4 kb[NPRE][4], vb[NPRE][4];
#define ISSUE_K(S, OFF) do { _Pragma("unroll") for (int i = 0; i < 4; ++i) kb[S][i] = bld_nt(kr, loff + i * 1024, (OFF)); __builtin_amdgcn_sched_barrier(0); } while (0)
#define ISSUE_V(S, OFF) do { _Pragma("unroll") for (int i = 0; i < 4; ++i) vb[S][i] = bld_nt(vr, loff + i * 1024, (OFF)); __builtin_amdgcn_sched_barrier(0); } while (0)
#define ISSUE(S, OFF) do { const unsigned o_ = (OFF); ISSUE_K(S, o_); ISSUE_V(S, o_); } while (0)
    if (wave >= NWD) {
      __builtin_amdgcn_s_setprio(3);
      const int row0 = 80 * x + 20 * (wave - NWD);
      rsrc_t wqr = __builtin_amdgcn_make_buffer_rsrc((void*)wq, 0, D_MODEL * NH * DH * 2, 0x00020000);
      const unsigned wq_voff = (unsigned)r * (NH * DH * 2) + (unsigned)c * 16;
      const unsigned wq_soff = (unsigned)row0 * (NH * DH * 2) + (unsigned)j * (DH * 2);
      unsigned xv[5]; u32x4 w[5];
#pragma unroll
      for (int i = 0; i < 5; ++i) xv[i] = xin[(row0 + 4 * i + r) >> 1];
#pragma unroll
      for (int i = 0; i < 5; ++i) w[i] = bld_nt(wqr, wq_voff, wq_soff + (unsigned)i * 4 * (NH * DH * 2));
      __builtin_amdgcn_sched_barrier(0);
      LDS_INIT_BARRIER();
      float qa[8];
#pragma unroll
      for (int k = 0; k < 8; ++k) qa[k] = 0.f;
#pragma unroll
      for (int i = 0; i < 5; ++i) {
        const unsigned xl = (r & 1) ? xv[i] >> 16 : xv[i] & 0xffffu, xh = xl << 16;
#pragma unroll
        for (int k = 0; k < 4; ++k) { qa[2 * k] = dot2(w[i][k], xl, qa[2 * k]); qa[2 * k + 1] = dot2(w[i][k], xh, qa[2 * k + 1]); }
      }
      *reinterpret_cast<f32x2*>(smem + L_QP + ((wave - NWD) * 128 + 8 * c + 2 * r) * 4) = treduce8(qa);
      asm volatile("s_waitcnt lgkmcnt(0)" ::: "memory");
      (void)lds_ticket(lbase + L_QCNT);
      __builtin_amdgcn_s_setprio(1);
      __builtin_amdgcn_sched_barrier(0);
      ISSUE_K(0, base1 + wave * TB);
    } else {
      ISSUE_K(0, base1 + wave * TB);
      LDS_INIT_BARRIER();
    }
    ISSUE_V(0, base1 + wave * TB);
#pragma unroll
    for (int s = 1; s < NPRE; ++s) ISSUE(s, base1 + (s * NWS + wave) * TB);
    const unsigned gen = __hip_atomic_load((gu32*)(ws + WS_GEN), RLX_AGENT);
    __builtin_amdgcn_sched_barrier(0);
    _Pragma("nounroll") for (unsigned spins = 0; spins < SPIN_L; ++spins) { if (lds_rd(lbase + L_QFLAG) != 0u) break; __builtin_amdgcn_s_sleep(1); }
    const u32x4 qv = *reinterpret_cast<const u32x4*>(smem + L_Q + c * 16);
    constexpr float C2 = 0.08838834764831845f * 1.4426950408889634f;
    float m = -INFINITY, l = 0.f, acc[8];
#pragma unroll
    for (int k = 0; k < 8; ++k) acc[k] = 0.f;
    bool dyn = false;
    auto next = [&]() -> unsigned {
      if (wave >= NWD) return ENDV;
      if (!dyn) {
        const unsigned t = lds_ticket(lbase + L_CNT1);
        if (t < (unsigned)(N1 - NPRE * NWS)) return base1 + (NPRE * NWS + t) * TB;
        dyn = true;
      }
      const unsigned u = lds_ticket(lbase + L_CNT2);
      if (u >= (unsigned)MAXU) return ENDV;
      unsigned v = ENDV;
      _Pragma("nounroll") for (unsigned spins = 0; spins < SPIN_L; ++spins) { v = lds_rd(lbase + L_TAB + 4 * u); if (v != NOTREADY) break; __builtin_amdgcn_s_sleep(1); }
      return v == NOTREADY ? ENDV : v;
    };
#define CONSUME(S) do { \
      float s_[4]; \
      _Pragma("unroll") for (int i = 0; i < 4; ++i) { float d_ = dot2(kb[S][i][0], qv[0], 0.f); d_ = dot2(kb[S][i][1], qv[1], d_); d_ = dot2(kb[S][i][2], qv[2], d_); d_ = dot2(kb[S][i][3], qv[3], d_); s_[i] = rowsum16(d_); } \
      const float mn_ = fmaxf(m, fmaxf(fmaxf(s_[0], s_[1]), fmaxf(s_[2], s_[3])) * C2); \
      const float al_ = __builtin_amdgcn_exp2f(m - mn_); m = mn_; l *= al_; \
      _Pragma("unroll") for (int k = 0; k < 8; ++k) acc[k] *= al_; \
      _Pragma("unroll") for (int i = 0; i < 4; ++i) { const float p_ = __builtin_amdgcn_exp2f(fmaf(s_[i], C2, -mn_)); l += p_; \
        _Pragma("unroll") for (int k = 0; k < 4; ++k) { acc[2 * k] = fmaf(p_, bf_lo(vb[S][i][k]), acc[2 * k]); acc[2 * k + 1] = fmaf(p_, bf_hi(vb[S][i][k]), acc[2 * k + 1]); } } \
      __builtin_amdgcn_sched_barrier(0); \
    } while (0)
    bool v0, v1;
    { const unsigned off = next(); CONSUME(0); v0 = off != ENDV; ISSUE(0, off); }
    { const unsigned off = next(); CONSUME(1); v1 = off != ENDV; ISSUE(1, off); }
#pragma unroll
    for (int s = 2; s < NPRE; ++s) CONSUME(s);
    _Pragma("nounroll") for (;;) {
      if (!v0) break;
      { const unsigned off = next(); CONSUME(0); v0 = off != ENDV; ISSUE(0, off); }
      if (!v1) break;
      { const unsigned off = next(); CONSUME(1); v1 = off != ENDV; ISSUE(1, off); }
    }
    tag = __builtin_amdgcn_readfirstlane(gen) + 1u;
    const float mw = xrow_max(m);
    const float sc = __builtin_amdgcn_exp2f(m - mw);
    const float lw = xrow_sum(l * sc);
#pragma unroll
    for (int k = 0; k < 8; ++k) acc[k] *= sc;
    *reinterpret_cast<f32x2*>(smem + L_REC + (wave * 128 + 8 * c + 2 * r) * 4) = treduce8(acc);
    if (lane < 2) reinterpret_cast<float*>(smem + L_ML)[lane * 8 + wave] = lane == 0 ? mw : lw;
    if (wave >= NWD && x < 5) {
      _Pragma("nounroll") for (unsigned spins = 0; spins < SPIN_L; ++spins) { if (lds_rd(lbase + L_ENDF) != 0u) break; __builtin_amdgcn_s_sleep(8); }
      rsrc_t wor = __builtin_amdgcn_make_buffer_rsrc((void*)wo, 0, NH * DH * D_MODEL * 2, 0x00020000);
      const unsigned voff = (unsigned)r * (D_MODEL * 2) + (unsigned)c * 16;
      const unsigned soff = (unsigned)(128 * j + 32 * (wave - NWD)) * (D_MODEL * 2) + (unsigned)x * 256;
      u32x4 t[8];
#pragma unroll
      for (int i = 0; i < 8; ++i) t[i] = bld_nt(wor, voff, soff + (unsigned)i * 4 * (D_MODEL * 2));
      __builtin_amdgcn_sched_barrier(0);
#pragma unroll
      for (int i = 0; i < 8; ++i) *reinterpret_cast<u32x4*>(smem + L_WO + ((wave - NWD) * 8 + i) * 1024 + lane * 16) = t[i];
    }
  }
  WG_BARRIER();
  if (tid < 144) {
    const f32x4 m0 = *reinterpret_cast<const f32x4*>(smem + L_ML), m1 = *reinterpret_cast<const f32x4*>(smem + L_ML + 16);
    const f32x4 l0 = *reinterpret_cast<const f32x4*>(smem + L_ML + 32), l1 = *reinterpret_cast<const f32x4*>(smem + L_ML + 48);
    const float* rec = reinterpret_cast<const float*>(smem + L_REC) + (tid & 127);
    float o[8];
#pragma unroll
    for (int w = 0; w < 8; ++w) o[w] = rec[w * 128];
    __builtin_amdgcn_sched_barrier(0);
    const float M = fmaxf(fmaxf(fmaxf(m0[0], m0[1]), fmaxf(m0[2], m0[3])), fmaxf(fmaxf(m1[0], m1[1]), fmaxf(m1[2], m1[3])));
    float L = 0.f, O = 0.f;
#pragma unroll
    for (int w = 0; w < 8; ++w) {
      const float e = __builtin_amdgcn_exp2f((w < 4 ? m0[w & 3] : m1[w & 3]) - M);
      L = fmaf(e, w < 4 ? l0[w & 3] : l1[w & 3], L); O = fmaf(e, o[w], O);
    }
    st_gran(ws + WS_PG + (size_t)(j * 8 + x) * 1152 + tid * 8, tag, tid < 128 ? O : (tid == 128 ? M : L));
  }
  if (x >= 5) return;
  u32x4 wv[4];
  unsigned long long pa0 = 0, pa1 = 0, pa2 = 0, pb0 = 0, pb1 = 0, pb2 = 0;
  if (wave < NWS) {
#pragma unroll
    for (int i = 0; i < 4; ++i) wv[i] = *reinterpret_cast<const u32x4*>(smem + L_WO + (wave * 4 + i) * 1024 + lane * 16);
    const unsigned char* pg = ws + WS_PG + (size_t)(j * 8 + wave) * 1152 + lane * 8;
    const unsigned char* pml = ws + WS_PG + (size_t)(j * 8 + wave) * 1152 + 1024 + (lane & 1) * 8;
    const unsigned which = poll3(pa0, pa1, pa2, pb0, pb1, pb2, pg, pml, tag);
    const unsigned long long g0 = which ? pb0 : pa0, g1 = which ? pb1 : pa1, gm = which ? pb2 : pa2;
    float* pgl = reinterpret_cast<float*>(smem + L_PG) + wave * 128;
    pgl[lane] = gval(g0); pgl[64 + lane] = gval(g1);
    if (lane < 2) reinterpret_cast<float*>(smem + L_ML2)[lane * 8 + wave] = gval(gm);
  }
  WG_BARRIER();
  if (wave < NWS) {
    const f32x2 mm = *reinterpret_cast<const f32x2*>(smem + L_ML2 + r * 8), ll = *reinterpret_cast<const f32x2*>(smem + L_ML2 + 32 + r * 8);
    const float* pgl = reinterpret_cast<const float*>(smem + L_PG) + (2 * r) * 128 + wave * 16 + c;
    const float oa = pgl[0], ob = pgl[128];
    const float M = xrow_max(fmaxf(mm[0], mm[1]));
    const float ea = __builtin_amdgcn_exp2f(mm[0] - M), eb = __builtin_amdgcn_exp2f(mm[1] - M);
    const float num = xrow_sum(fmaf(ea, oa, eb * ob)), den = xrow_sum(fmaf(ea, ll[0], eb * ll[1]));
    const unsigned obf = f2bf(num * __builtin_amdgcn_rcpf(den));
    float a[8];
#pragma unroll
    for (int k = 0; k < 8; ++k) a[k] = 0.f;
#pragma unroll
    for (int i = 0; i < 4; ++i) {
      const unsigned ol = (unsigned)__builtin_amdgcn_ds_bpermute((4 * i + r) * 4, (int)obf), oh = ol << 16;
#pragma unroll
      for (int k = 0; k < 4; ++k) { a[2 * k] = dot2(wv[i][k], ol, a[2 * k]); a[2 * k + 1] = dot2(wv[i][k], oh, a[2 * k + 1]); }
    }
    *reinterpret_cast<f32x2*>(smem + L_YP + (wave * 128 + 8 * c + 2 * r) * 4) = treduce8(a);
    POLL_DRAIN3(pa0, pa1, pa2, pb0, pb1, pb2);
  }
  WG_BARRIER();
  if (tid < 128) {
    const float* yp = reinterpret_cast<const float*>(smem + L_YP);
    float y = 0.f;
#pragma unroll
    for (int w = 0; w < 8; ++w) y += yp[w * 128 + tid];
    st_gran(ws + WS_YG + (((size_t)x * 32 + j) * 128 + tid) * 8, tag, y);
  }
  if (wave == 2) {
    const unsigned char* yg = ws + WS_YG + (((size_t)x * 32 + (lane >> 1)) * 128 + 4 * j + (lane & 1) * 2) * 8;
    unsigned long long a0, a1, b0, b1;
    const unsigned which = poll2(a0, a1, b0, b1, yg, tag);
    const unsigned long long g0 = which ? b0 : a0, g1 = which ? b1 : a1;
    float y0 = gval(g0), y1 = gval(g1);
    y0 += dpp<0x4E>(y0); y1 += dpp<0x4E>(y1);
    y0 += dpp<0x124>(y0); y1 += dpp<0x124>(y1);
    y0 += dpp<0x128>(y0); y1 += dpp<0x128>(y1);
    y0 = xrow_sum(y0); y1 = xrow_sum(y1);
    if (lane < 2) out[64 * x + 2 * j + lane] = f2bf(y0) | (f2bf(y1) << 16);
    if (b == 0 && lane == 0) __hip_atomic_store((gu32*)(ws + WS_GEN), tag, RLX_AGENT);
    POLL_DRAIN2(a0, a1, b0, b1);
  }
}

extern "C" void kernel_launch(void* const* d_in, const int* in_sizes, int n_in, void* d_out, int out_size, void* d_ws, size_t ws_size, hipStream_t stream) {
  (void)in_sizes; (void)n_in; (void)out_size; (void)ws_size;
  (void)hipFuncSetAttribute((const void*)gqa_decode_kernel, hipFuncAttributeMaxDynamicSharedMemorySize, LDS_BYTES);
  gqa_decode_kernel<<<256, NTHR, LDS_BYTES, stream>>>((const unsigned*)d_in[0], (const unsigned char*)d_in[1], (const unsigned char*)d_in[2],
                                                     (const unsigned char*)d_in[3], (const unsigned char*)d_in[4], (unsigned*)d_out, (unsigned char*)d_ws);
}
```
